```python
import torch

def _softmax(x, axis=-1):
    return torch.softmax(x, dim=axis)
def _gelu(x):
    return torch.nn.functional.gelu(x, approximate="tanh")
def _silu(x):
    return torch.nn.functional.silu(x)
def _sigmoid(x):
    return torch.sigmoid(x)
def _layernorm(x, axis=-1, eps=1e-5):
    m = x.mean(dim=axis, keepdim=True)
    v = x.var(dim=axis, keepdim=True, unbiased=False)
    return (x - m) / torch.sqrt(v + eps)
def _scatter_add(tgt, idx, src, axis=0):
    return tgt.clone().index_add_(axis, idx, src)
def _segment_sum(src, seg, n):
    out = torch.zeros((n,) + src.shape[1:], dtype=src.dtype)
    return out.index_add_(0, seg, src)
def _pad(x, pw):
    flat = tuple(v for pair in reversed(pw) for v in pair)
    return torch.nn.functional.pad(x, flat)

kernel_name = 'parallel_sum__ssm_conv_h3__glu_gated_swiglu__prompt'

def setup_inputs():

    _w = (lambda a: (lambda t: t.to(torch.bfloat16) if t.is_floating_point() else t)(torch.as_tensor(a)))
    def _A_setup():
        B, S, D = 1, 2048, 576
        ks = 4
        return {
            "q": _w((lambda *s: torch.randn(*s, dtype=torch.bfloat16) * 0.05)(B, S, D)),
            "k": _w((lambda *s: torch.randn(*s, dtype=torch.bfloat16) * 0.2)(B, S, D)),
            "v": _w((lambda *s: torch.randn(*s, dtype=torch.bfloat16) * 0.2)(B, S, D)),
            "shift_k": _w((lambda *s: torch.randn(*s, dtype=torch.bfloat16) * 0.2)(D, ks) * 0.5),
            "ssm_k": _w((lambda *s: torch.randn(*s, dtype=torch.bfloat16) * 0.4)(D, S) * (1.0 / S ** 0.5)),
            "ssm_D": _w((lambda *s: torch.randn(*s, dtype=torch.bfloat16) * 0.2)(D)),
        }
    _A_in = _A_setup()

    def _B_setup():
        B, S, D, F = 1, 2048, 576, 4608
        return {
            "x": _w((lambda *s: torch.randn(*s, dtype=torch.bfloat16) * 0.05)(B, S, D)),
            "w1": _w((lambda *s: torch.randn(*s, dtype=torch.bfloat16) * 0.2)(D, F) * (2.0 / D) ** 0.5),
            "w3": _w((lambda *s: torch.randn(*s, dtype=torch.bfloat16) * 0.2)(D, F) * (2.0 / D) ** 0.5),
            "w2": _w((lambda *s: torch.randn(*s, dtype=torch.bfloat16) * 0.2)(F, D) * (2.0 / F) ** 0.5),
        }
    _B_in = _B_setup()

    return {
        "A_q": _A_in["q"],
        "A_k": _A_in["k"],
        "A_v": _A_in["v"],
        "A_shift_k": _A_in["shift_k"],
        "A_ssm_k": _A_in["ssm_k"],
        "A_ssm_D": _A_in["ssm_D"],
        "B_x": _B_in["x"],
        "B_w1": _B_in["w1"],
        "B_w3": _B_in["w3"],
        "B_w2": _B_in["w2"],
    }

def reference(A_q, A_k, A_v, A_shift_k, A_ssm_k, A_ssm_D, B_x, B_w1, B_w3, B_w2):
    def _A_ref(q, k, v, shift_k, ssm_k, ssm_D):
        B, S, D = q.shape
        ks = shift_k.shape[-1]
        k_pad = _pad(k, ((0, 0), (ks - 1, 0), (0, 0)))
        k_shift = torch.zeros_like(k)
        for t in range(ks):
            k_shift = k_shift + k_pad[:, t:t + S, :] * shift_k[:, ks - 1 - t][None, None, :]
        kv = k_shift * v
        L = 2 * S
        kv_pad = _pad(kv, ((0, 0), (0, S), (0, 0)))
        kf_pad = _pad(ssm_k, ((0, 0), (0, S)))
        kv_f = (lambda x, axes=(-2,-1): torch.fft.rfft2(x.float(), dim=axes))(kv_pad[:, None, :, :], axes=(1, 2))
        kf_f = (lambda x, axes=(-2,-1): torch.fft.rfft2(x.float(), dim=axes))(kf_pad[:, None, :], axes=(1, 2))
        kf_f = (lambda x, *d: x.permute(*d))(kf_f, 1, 2, 0)
        y_f = kv_f * kf_f[None, :, :, :]
        y = (lambda x, s, axes=(-2,-1): torch.fft.irfft2(x, s=s, dim=axes).to(torch.bfloat16))(y_f, (1, L), axes=(1, 2))[:, 0, :S, :]
        y = y + kv * ssm_D[None, None, :]
        return q * y

    def _B_ref(x, w1, w3, w2):
        gate = torch.einsum("bsd,df->bsf", x, w1)
        up = torch.einsum("bsd,df->bsf", x, w3)
        h = _silu(gate) * up
        out = torch.einsum("bsf,fd->bsd", h, w2)
        return out

    x = A_q
    xn = _layernorm(x, axis=-1)
    return x + _A_ref(xn, A_k, A_v, A_shift_k, A_ssm_k, A_ssm_D) + _B_ref(xn, B_w1, B_w3, B_w2)
```

```cpp
#include <hip/hip_runtime.h>
#include <hip/hip_bf16.h>
#include <stdint.h>

typedef unsigned short u16;
typedef __bf16 bf16x8 __attribute__((ext_vector_type(8)));
typedef __bf16 bf16x2 __attribute__((ext_vector_type(2)));
typedef short s16x4 __attribute__((ext_vector_type(4)));
typedef short s16x8 __attribute__((ext_vector_type(8)));
typedef float f32x4 __attribute__((ext_vector_type(4)));
typedef unsigned u32x4 __attribute__((ext_vector_type(4)));
typedef unsigned u32x2 __attribute__((ext_vector_type(2)));
#define LDSP(T) __attribute__((address_space(3))) T*

constexpr int S_ = 2048, D_ = 576, F_ = 4608;
constexpr int RP = 2064;

constexpr size_t WS_XNF = 0;
constexpr size_t WS_STATS = WS_XNF + (size_t)S_ * D_ * 2;
constexpr size_t WS_R = WS_STATS + (size_t)S_ * 8;
constexpr size_t WS_ZT = WS_R + (size_t)D_ * RP * 2;
constexpr size_t WS_HF = WS_ZT + (size_t)D_ * S_ * 2;
constexpr size_t WS_PART = WS_HF + (size_t)S_ * F_ * 2;

__device__ __forceinline__ float b2f(unsigned h) { return __uint_as_float(h << 16); }
__device__ __forceinline__ float lo2f(unsigned w) { return __uint_as_float(w << 16); }
__device__ __forceinline__ float hi2f(unsigned w) { return __uint_as_float(w & 0xffff0000u); }
__device__ __forceinline__ float rb(float f) { return (float)(__bf16)f; }
__device__ __forceinline__ unsigned pack2(float lo, float hi) {
  bf16x2 v; v[0] = (__bf16)lo; v[1] = (__bf16)hi; return __builtin_bit_cast(unsigned, v);
}

__global__ __launch_bounds__(384) void k_prep(const u16* __restrict__ x, const u16* __restrict__ kk,
                                              const u16* __restrict__ vv, const u16* __restrict__ sk,
                                              u16* __restrict__ xnf, float2* __restrict__ stats, u16* __restrict__ R) {
  __shared__ __attribute__((aligned(16))) unsigned sm[96 * 33];
  const int tid = threadIdx.x, b = blockIdx.x;
  if (b < 128) {
    float* red = (float*)sm;
    const int w = tid >> 6, l = tid & 63, m = l & 15, g = l >> 4;
    const int row = 16 * b + m;
    float v[24];
#pragma unroll
    for (int i = 0; i < 3; ++i) {
      const int ks = 3 * w + i;
      const u16* p = x + row * D_ + 32 * ks + 4 * g;
      u32x2 a = *(const u32x2*)p, c = *(const u32x2*)(p + 16);
      v[8 * i + 0] = lo2f(a[0]); v[8 * i + 1] = hi2f(a[0]); v[8 * i + 2] = lo2f(a[1]); v[8 * i + 3] = hi2f(a[1]);
      v[8 * i + 4] = lo2f(c[0]); v[8 * i + 5] = hi2f(c[0]); v[8 * i + 6] = lo2f(c[1]); v[8 * i + 7] = hi2f(c[1]);
    }
    float s = 0.f;
#pragma unroll
    for (int i = 0; i < 24; ++i) s += v[i];
    s += __shfl_xor(s, 16); s += __shfl_xor(s, 32);
    if (l < 16) red[w * 16 + m] = s;
    __syncthreads();
    float tot = 0.f;
#pragma unroll
    for (int i = 0; i < 6; ++i) tot += red[i * 16 + m];
    const float mean = tot * (1.0f / D_);
    float q = 0.f;
#pragma unroll
    for (int i = 0; i < 24; ++i) { float d = v[i] - mean; q += d * d; }
    q += __shfl_xor(q, 16); q += __shfl_xor(q, 32);
    if (l < 16) red[96 + w * 16 + m] = q;
    __syncthreads();
    float qt = 0.f;
#pragma unroll
    for (int i = 0; i < 6; ++i) qt += red[96 + i * 16 + m];
    const float mb = rb(mean), vb = rb(qt * (1.0f / D_));
    const float sd = rb(sqrtf(rb(vb + 1e-5f)));
    if (tid < 16) stats[row] = make_float2(mb, sd);
#pragma unroll
    for (int i = 0; i < 3; ++i) {
      const int ks = 3 * w + i;
      u32x4 o;
#pragma unroll
      for (int j = 0; j < 4; ++j)
        o[j] = pack2(rb(v[8 * i + 2 * j] - mb) / sd, rb(v[8 * i + 2 * j + 1] - mb) / sd);
      *(u32x4*)(xnf + ((size_t)(b * 18 + ks) * 64 + l) * 8) = o;
    }
  } else {
    const int bb = b - 128, tb = bb / 6, cb = bb % 6;
    const int t0 = 64 * tb, c0 = 96 * cb;
    const int cg = tid % 12, tp = tid / 12;
    const int t = t0 + 2 * tp, c = c0 + 8 * cg;
    u32x4 kr[5], vr[2], sr[4];
#pragma unroll
    for (int i = 0; i < 5; ++i) {
      const int tt = t - 3 + i;
      kr[i] = (tt >= 0) ? *(const u32x4*)(kk + tt * D_ + c) : (u32x4){0u, 0u, 0u, 0u};
    }
    vr[0] = *(const u32x4*)(vv + t * D_ + c);
    vr[1] = *(const u32x4*)(vv + (t + 1) * D_ + c);
#pragma unroll
    for (int i = 0; i < 4; ++i) sr[i] = *(const u32x4*)(sk + c * 4 + 8 * i);
#pragma unroll
    for (int e = 0; e < 8; ++e) {
      const unsigned s01 = sr[e >> 1][(e & 1) * 2], s23 = sr[e >> 1][(e & 1) * 2 + 1];
      const float s0 = lo2f(s01), s1 = hi2f(s01), s2 = lo2f(s23), s3 = hi2f(s23);
      float kf[5];
#pragma unroll
      for (int i = 0; i < 5; ++i) kf[i] = (e & 1) ? hi2f(kr[i][e >> 1]) : lo2f(kr[i][e >> 1]);
      float r2[2];
#pragma unroll
      for (int u = 0; u < 2; ++u) {
        float a = rb(kf[u] * s3);
        a = rb(a + rb(kf[u + 1] * s2));
        a = rb(a + rb(kf[u + 2] * s1));
        a = rb(a + rb(kf[u + 3] * s0));
        const float vf = (e & 1) ? hi2f(vr[u][e >> 1]) : lo2f(vr[u][e >> 1]);
        r2[u] = a * vf;
      }
      sm[(8 * cg + e) * 33 + (31 - tp)] = pack2(r2[1], r2[0]);
    }
    __syncthreads();
    const int u0 = 1984 - t0;
#pragma unroll
    for (int i = 0; i < 2; ++i) {
      const int id = tid + 384 * i, r = id >> 3, ch = id & 7;
      u32x4 o;
#pragma unroll
      for (int j = 0; j < 4; ++j) o[j] = sm[r * 33 + ch * 4 + j];
      *(u32x4*)(R + (size_t)(c0 + r) * RP + u0 + ch * 8) = o;
    }
    if (tb == 0 && tid < 192) {
      *(u32x4*)(R + (size_t)(c0 + (tid >> 1)) * RP + 2048 + (tid & 1) * 8) = (u32x4){0u, 0u, 0u, 0u};
    }
  }
}

constexpr int CS = 4160, HB = 8 * CS;
__global__ __launch_bounds__(256) void k_conv(const u16* __restrict__ R, const u16* __restrict__ hk,
                                              const u16* __restrict__ Dp, u16* __restrict__ zT) {
  __shared__ __attribute__((aligned(16))) char sm[HB + 4608];
  const int tid = threadIdx.x, c = blockIdx.x;
  const u16* Rc = R + (size_t)c * RP;
  for (int j = tid; j < 257; j += 256) {
    u32x4 a = *(const u32x4*)(Rc + 8 * j), bq = *(const u32x4*)(Rc + 8 * j + 8);
    unsigned d[8] = {a[0], a[1], a[2], a[3], bq[0], bq[1], bq[2], bq[3]};
    unsigned e[7];
#pragma unroll
    for (int i = 0; i < 7; ++i) e[i] = __builtin_amdgcn_alignbyte(d[i + 1], d[i], 2);
#pragma unroll
    for (int sg = 0; sg < 8; ++sg) {
      u32x4 o;
#pragma unroll
      for (int i = 0; i < 4; ++i) o[i] = (sg & 1) ? e[(sg >> 1) + i] : d[(sg >> 1) + i];
      *(u32x4*)(sm + (7 - sg) * CS + 16 * j) = o;
    }
  }
  *(u32x4*)(sm + HB + 480 + 16 * tid) = *(const u32x4*)(hk + (size_t)c * S_ + 8 * tid);
  if (tid < 30) *(u32x4*)(sm + HB + 16 * tid) = (u32x4){0u, 0u, 0u, 0u};
  const float dval = b2f(Dp[c]);
  __syncthreads();
  const int w = tid >> 6, l = tid & 63, r = l & 15, kg = l >> 4, rl = r & 7, rh = r >> 3;
  f32x4 acc[8];
#pragma unroll
  for (int i = 0; i < 8; ++i) acc[i] = (f32x4){0.f, 0.f, 0.f, 0.f};
#pragma unroll
  for (int rr = 0; rr < 2; ++rr) {
    const int rho = 4 * w + 1 + 2 * rr;
    const char* pa = sm + rl * CS + 4080 - 32 * rho - 16 * rh + 16 * kg;
    const char* pb = sm + HB + 480 - 32 * rho + 32 * r + 16 * kg;
    bf16x8 A[8], B[8];
#pragma unroll
    for (int i = 0; i < 8; ++i) { A[i] = *(const bf16x8*)(pa - 512 * i); B[i] = *(const bf16x8*)(pb + 512 * i); }
#pragma unroll
    for (int a = 0; a < 8; ++a)
#pragma unroll
      for (int bb = 0; bb < 8 - a; ++bb)
        acc[a + bb] = __builtin_amdgcn_mfma_f32_16x16x32_bf16(A[a], B[bb], acc[a + bb], 0, 0, 0);
  }
  u32x2 kvr[2];
#pragma unroll
  for (int i = 0; i < 2; ++i) {
    const int t = 16 * (16 * (2 * w + i) + r) + 4 * kg;
    kvr[i] = *(const u32x2*)(sm + 7 * CS + 2 * (2044 - t));
  }
  __syncthreads();
#pragma unroll
  for (int i = 0; i < 8; ++i) *(f32x4*)(sm + ((w * 8 + i) * 64 + l) * 16) = acc[i];
  __syncthreads();
#pragma unroll
  for (int i = 0; i < 2; ++i) {
    const int ct = 2 * w + i;
    f32x4 s = *(const f32x4*)(sm + ((0 * 8 + ct) * 64 + l) * 16);
#pragma unroll
    for (int ww = 1; ww < 4; ++ww) s += *(const f32x4*)(sm + ((ww * 8 + ct) * 64 + l) * 16);
    const float k0 = hi2f(kvr[i][1]), k1 = lo2f(kvr[i][1]), k2 = hi2f(kvr[i][0]), k3 = lo2f(kvr[i][0]);
    u32x2 o;
    o[0] = pack2(rb(s[0]) + rb(k0 * dval), rb(s[1]) + rb(k1 * dval));
    o[1] = pack2(rb(s[2]) + rb(k2 * dval), rb(s[3]) + rb(k3 * dval));
    const int t = 16 * (16 * ct + r) + 4 * kg;
    *(u32x2*)(zT + (size_t)c * S_ + t) = o;
  }
}

__device__ __forceinline__ void dma16(const void* sbase, unsigned voff, unsigned lds_dst) {
  unsigned keep;
  asm volatile("s_mov_b32 %0, m0\n\ts_mov_b32 m0, %3\n\ts_nop 0\n\tglobal_load_lds_dwordx4 %1, %2\n\ts_mov_b32 m0, %0"
               : "=&s"(keep) : "v"(voff), "s"(sbase), "s"(lds_dst) : "memory");
}
__device__ __forceinline__ void gload16(u32x4& dst, const void* sbase, unsigned voff) {
  asm volatile("global_load_dwordx4 %0, %1, %2" : "=v"(dst) : "v"(voff), "s"(sbase));
}
#define WAITV4(n, a, b, c, d) asm volatile("s_waitcnt vmcnt(%4)" : "+v"(a), "+v"(b), "+v"(c), "+v"(d) : "n"(n) : "memory")

constexpr int PANEL = 64 * 288, UNIT = 2 * PANEL, NSLOT = 4;

__device__ __forceinline__ bf16x8 ldb(unsigned addr) {
  s16x4 lo = __builtin_amdgcn_ds_read_tr16_b64_v4i16((LDSP(s16x4))addr);
  s16x4 hi = __builtin_amdgcn_ds_read_tr16_b64_v4i16((LDSP(s16x4))(addr + 16 * 288));
  s16x8 v = __builtin_shufflevector(lo, hi, 0, 1, 2, 3, 4, 5, 6, 7);
  return __builtin_bit_cast(bf16x8, v);
}

__device__ __forceinline__ void dma16_half(const void* sbase, unsigned voff, unsigned lds_dst) {
  unsigned keep;
  asm volatile("s_mov_b32 %0, m0\n\ts_mov_b32 m0, %3\n\ts_mov_b32 exec_hi, 0\n\tglobal_load_lds_dwordx4 %1, %2\n\ts_mov_b32 exec_hi, -1\n\ts_mov_b32 m0, %0"
               : "=&s"(keep) : "v"(voff), "s"(sbase), "s"(lds_dst) : "memory");
}
#define SCHED() __builtin_amdgcn_sched_barrier(0)

template <int NP>
__device__ __forceinline__ void gemm_main(f32x4 (&acc)[2][9 * NP], const char* bsrc  ,
                                          unsigned bstride  , const char* asrc, unsigned aoff0,
                                          unsigned aoff1, unsigned lbase, int w, int l, int mypanel) {
  constexpr int NIT = 9, NT = 9 * NP, PD = 3, NQ = 4;
  unsigned doff[5];
#pragma unroll
  for (int j = 0; j < 5; ++j) {
    const int i = (j < 4) ? 64 * j + l : 256 + (l & 31);
    const int row = i / 18, ch = i - row * 18;
    doff[j] = (unsigned)((w & 3) * 16 + row) * bstride + ch * 16;
  }
  const unsigned ldst = lbase + (w >> 2) * PANEL + (w & 3) * (16 * 288);
  auto issue_dma1 = [&](int u, int j) {
    const char* src = bsrc + (size_t)u * 64 * bstride;
    const unsigned dst = ldst + (u % NSLOT) * UNIT + j * 1024;
    if (j < 4) dma16(src, doff[j], dst); else dma16_half(src, doff[4], dst);
  };
  u32x4 A[3][4];
  auto issue_a1 = [&](int u, int j) {
    const char* src = asrc + (size_t)u * 2048 + (j >> 1) * 1024;
    gload16(A[u % 3][j], src, (j & 1) ? aoff1 : aoff0);
  };
  const int g = l >> 4, q = (l >> 2) & 3, p = l & 3;
  const unsigned rbase = lbase + (4 * g + q) * 288 + 8 * p + (NP == 1 ? mypanel * PANEL : 0);
  bf16x8 bq[NQ];
  auto ldstep = [&](int i) {
    const int kst = i / NT, t = i % NT, u = kst >> 1, ks = kst & 1;
    bq[i % NQ] = ldb(rbase + (u % NSLOT) * UNIT + ks * 32 * 288 + (t / 9) * PANEL + (t % 9) * 32);
  };
#pragma unroll
  for (int j = 0; j < 5; ++j) issue_dma1(0, j);
#pragma unroll
  for (int j = 0; j < 4; ++j) issue_a1(0, j);
#pragma unroll
  for (int j = 0; j < 5; ++j) issue_dma1(1, j);
#pragma unroll
  for (int j = 0; j < 4; ++j) issue_a1(1, j);
#pragma unroll
  for (int j = 0; j < 5; ++j) issue_dma1(2, j);
  WAITV4(14, A[0][0], A[0][1], A[0][2], A[0][3]);
  __builtin_amdgcn_s_barrier();
#pragma unroll
  for (int i = 0; i < PD; ++i) ldstep(i);
  SCHED();
#pragma unroll
  for (int kst = 0; kst < 2 * NIT; ++kst) {
    const int u = kst >> 1, ks = kst & 1;
    if (ks == 1 && u + 1 < NIT) {
      if (u + 2 < NIT) { WAITV4(5, A[(u + 1) % 3][0], A[(u + 1) % 3][1], A[(u + 1) % 3][2], A[(u + 1) % 3][3]); }
      else { WAITV4(0, A[(u + 1) % 3][0], A[(u + 1) % 3][1], A[(u + 1) % 3][2], A[(u + 1) % 3][3]); }
      __builtin_amdgcn_s_barrier();
      SCHED();
    }
#pragma unroll
    for (int t = 0; t < NT; ++t) {
      const int i = kst * NT + t;
      if (i + PD < 2 * NIT * NT) ldstep(i + PD);
      if (ks == 1) {
        if (t < 4 && u + 2 < NIT) issue_a1(u + 2, t);
        if (t >= 4 && t < 9 && u + 3 < NIT) issue_dma1(u + 3, t - 4);
      }
      const bf16x8 a0 = __builtin_bit_cast(bf16x8, A[u % 3][2 * ks]);
      const bf16x8 a1 = __builtin_bit_cast(bf16x8, A[u % 3][2 * ks + 1]);
      acc[0][t] = __builtin_amdgcn_mfma_f32_16x16x32_bf16(bq[i % NQ], a0, acc[0][t], 0, 0, 0);
      acc[1][t] = __builtin_amdgcn_mfma_f32_16x16x32_bf16(bq[i % NQ], a1, acc[1][t], 0, 0, 0);
      SCHED();
    }
  }
}

__global__ __launch_bounds__(512, 2) void k_gemm1(const u16* __restrict__ xnf, const u16* __restrict__ w1,
                                                  const u16* __restrict__ w3, u16* __restrict__ hf) {
  __shared__ __attribute__((aligned(16))) char sm[NSLOT * UNIT];
  const unsigned lbase = (unsigned)(uintptr_t)(LDSP(char))sm;
  const int tid = threadIdx.x, l = tid & 63;
  const int w = __builtin_amdgcn_readfirstlane(tid >> 6);
  const int xcd = blockIdx.x & 7, idx = blockIdx.x >> 3;
  const int cb = 4 * xcd + (idx & 3), rbk = idx >> 2;
  f32x4 acc[2][18];
#pragma unroll
  for (int i = 0; i < 2; ++i)
#pragma unroll
    for (int j = 0; j < 18; ++j) acc[i][j] = (f32x4){0.f, 0.f, 0.f, 0.f};
  const char* bsrc = (const char*)((w >> 2) ? w3 : w1) + cb * 288;
  const int mt0 = 16 * rbk + 2 * w;
  gemm_main<2>(acc, bsrc, F_ * 2, (const char*)xnf + (size_t)mt0 * 18432, l * 16, l * 16 + 18432, lbase, w, l, 0);
#pragma unroll
  for (int i = 0; i < 2; ++i) {
#pragma unroll
    for (int t = 0; t < 9; ++t) {
      float hv[4];
#pragma unroll
      for (int j = 0; j < 4; ++j) {
        const float gt = rb(acc[i][t][j]), up = rb(acc[i][9 + t][j]);
        const float sg = rb(gt * __builtin_amdgcn_rcpf(1.0f + __builtin_amdgcn_exp2f(gt * -1.4426950408889634f)));
        hv[j] = sg * up;
      }
      u32x2 o; o[0] = pack2(hv[0], hv[1]); o[1] = pack2(hv[2], hv[3]);
      const int ft = 9 * cb + t;
      *(u32x2*)(hf + ((size_t)((mt0 + i) * 144 + (ft >> 1)) * 64 + l) * 8 + (ft & 1) * 4) = o;
    }
  }
}

__global__ __launch_bounds__(512, 2) void k_gemm2(const u16* __restrict__ hf, const u16* __restrict__ w2,
                                                  float* __restrict__ part) {
  __shared__ __attribute__((aligned(16))) char sm[NSLOT * UNIT];
  const unsigned lbase = (unsigned)(uintptr_t)(LDSP(char))sm;
  const int tid = threadIdx.x, l = tid & 63;
  const int w = __builtin_amdgcn_readfirstlane(tid >> 6);
  const int xcd = blockIdx.x & 7, idx = blockIdx.x >> 3;
  const int sp = xcd >> 1, cb = idx & 3, rbk = 8 * (xcd & 1) + (idx >> 2);
  const int mw = w & 3, kh = w >> 2;
  f32x4 acc[2][9];
#pragma unroll
  for (int i = 0; i < 2; ++i)
#pragma unroll
    for (int j = 0; j < 9; ++j) acc[i][j] = (f32x4){0.f, 0.f, 0.f, 0.f};
  const char* bsrc = (const char*)w2 + (size_t)(sp * 1152 + kh * 576) * (D_ * 2) + cb * 288;
  const int mt0 = 8 * rbk + 2 * mw;
  const char* asrc = (const char*)hf + ((size_t)mt0 * 144 + sp * 36 + kh * 18) * 1024;
  gemm_main<1>(acc, bsrc, D_ * 2, asrc, l * 16, l * 16 + 144 * 1024, lbase, w, l, kh);
  __syncthreads();
  f32x4* red = (f32x4*)sm;
#pragma unroll
  for (int t = 0; t < 9; ++t) red[((w * 9) + t) * 64 + l] = kh ? acc[0][t] : acc[1][t];
  __syncthreads();
  const int pw = w ^ 4;
  const int m = 16 * (mt0 + kh) + (l & 15);
  float* dst = part + ((size_t)sp * S_ + m) * D_ + cb * 144 + 4 * (l >> 4);
#pragma unroll
  for (int t = 0; t < 9; ++t) {
    f32x4 v = (kh ? acc[1][t] : acc[0][t]) + red[((pw * 9) + t) * 64 + l];
    *(f32x4*)(dst + 16 * t) = v;
  }
}

__global__ __launch_bounds__(256) void k_final(const u16* __restrict__ x, const float2* __restrict__ stats,
                                               const u16* __restrict__ zT, const float* __restrict__ part,
                                               u16* __restrict__ out) {
  __shared__ __attribute__((aligned(16))) u16 zs[64 * 66];
  const int tid = threadIdx.x, tb = blockIdx.x / 9, cb = blockIdx.x % 9;
  const int t0 = 64 * tb, c0 = 64 * cb;
#pragma unroll
  for (int i = 0; i < 2; ++i) {
    const int id = tid + 256 * i, r = id >> 3, ch = id & 7;
    u32x4 v = *(const u32x4*)(zT + (size_t)(c0 + r) * S_ + t0 + ch * 8);
    unsigned* d = (unsigned*)(zs + r * 66 + ch * 8);
#pragma unroll
    for (int j = 0; j < 4; ++j) d[j] = v[j];
  }
  f32x4 pa[2][2];
  u32x4 xr[2];
  float2 st[2];
#pragma unroll
  for (int i = 0; i < 2; ++i) {
    const int id = tid + 256 * i, r = id >> 3, ch = id & 7;
    const size_t o = (size_t)(t0 + r) * D_ + c0 + ch * 8;
    pa[i][0] = *(const f32x4*)(part + o); pa[i][1] = *(const f32x4*)(part + o + 4);
#pragma unroll
    for (int s = 1; s < 4; ++s) {
      pa[i][0] += *(const f32x4*)(part + (size_t)s * S_ * D_ + o);
      pa[i][1] += *(const f32x4*)(part + (size_t)s * S_ * D_ + o + 4);
    }
    xr[i] = *(const u32x4*)(x + o);
    st[i] = stats[t0 + r];
  }
  __syncthreads();
#pragma unroll
  for (int i = 0; i < 2; ++i) {
    const int id = tid + 256 * i, r = id >> 3, ch = id & 7;
    float res[8];
#pragma unroll
    for (int e = 0; e < 8; ++e) {
      const float xv = (e & 1) ? hi2f(xr[i][e >> 1]) : lo2f(xr[i][e >> 1]);
      const float xn = rb(rb(xv - st[i].x) / st[i].y);
      const float z = b2f(zs[(ch * 8 + e) * 66 + r]);
      const float r1 = rb(xv + rb(xn * z));
      res[e] = r1 + rb(pa[i][e >> 2][e & 3]);
    }
    u32x4 o;
#pragma unroll
    for (int j = 0; j < 4; ++j) o[j] = pack2(res[2 * j], res[2 * j + 1]);
    *(u32x4*)(out + (size_t)(t0 + r) * D_ + c0 + ch * 8) = o;
  }
}

extern "C" void kernel_launch(void* const* d_in, const int* in_sizes, int n_in, void* d_out, int out_size,
                              void* d_ws, size_t ws_size, hipStream_t stream) {
  const u16* x = (const u16*)d_in[0];
  const u16* kk = (const u16*)d_in[1];
  const u16* vv = (const u16*)d_in[2];
  const u16* sk = (const u16*)d_in[3];
  const u16* hk = (const u16*)d_in[4];
  const u16* Dp = (const u16*)d_in[5];
  const u16* w1 = (const u16*)d_in[7];
  const u16* w3 = (const u16*)d_in[8];
  const u16* w2 = (const u16*)d_in[9];
  char* ws = (char*)d_ws;
  u16* xnf = (u16*)(ws + WS_XNF);
  float2* stats = (float2*)(ws + WS_STATS);
  u16* R = (u16*)(ws + WS_R);
  u16* zT = (u16*)(ws + WS_ZT);
  u16* hf = (u16*)(ws + WS_HF);
  float* part = (float*)(ws + WS_PART);
  k_prep<<<320, 384, 0, stream>>>(x, kk, vv, sk, xnf, stats, R);
  k_conv<<<576, 256, 0, stream>>>(R, hk, Dp, zT);
  k_gemm1<<<256, 512, 0, stream>>>(xnf, w1, w3, hf);
  k_gemm2<<<256, 512, 0, stream>>>(hf, w2, part);
  k_final<<<288, 256, 0, stream>>>(x, stats, zT, part, (u16*)d_out);
}
```

```cpp
#include <hip/hip_runtime.h>
#include <hip/hip_bf16.h>
#include <stdint.h>
#define ST_DECL()
#define STAMP(i)
#define SUB(k, i)

typedef unsigned short u16;
typedef __bf16 bf16x8 __attribute__((ext_vector_type(8)));
typedef __bf16 bf16x2 __attribute__((ext_vector_type(2)));
typedef short s16x4 __attribute__((ext_vector_type(4)));
typedef float f32x4 __attribute__((ext_vector_type(4)));
typedef unsigned u32x4 __attribute__((ext_vector_type(4)));
typedef unsigned u32x2 __attribute__((ext_vector_type(2)));
#define LDSP(T) __attribute__((address_space(3))) T*
#define GASP(T) __attribute__((address_space(1))) T*

constexpr int S_ = 2048, D_ = 576, F_ = 4608;
constexpr int RP = 2064;

constexpr size_t WS_CTL = 0;
constexpr int FL_X = 256, FL_KV = 512, FL_H = 768, FL_Z = 1024, FL_P = 1280;
constexpr size_t WS_XNF = 16384;
constexpr size_t WS_R0 = WS_XNF + (size_t)S_ * D_ * 2;
constexpr size_t WS_R1 = WS_R0 + (size_t)D_ * RP * 2;
constexpr size_t WS_ZT = WS_R1 + (size_t)D_ * RP * 2;
constexpr size_t WS_HF = WS_ZT + (size_t)D_ * S_ * 2;
constexpr size_t WS_SLAB = WS_HF + (size_t)S_ * F_ * 2;
constexpr size_t WS_SLABQ = WS_SLAB + (size_t)64 * 2 * 4 * 3 * 8192;

__device__ __forceinline__ float lo2f(unsigned w) { return __uint_as_float(w << 16); }
__device__ __forceinline__ float hi2f(unsigned w) { return __uint_as_float(w & 0xffff0000u); }
__device__ __forceinline__ float rb(float f) { return (float)(__bf16)f; }
__device__ __forceinline__ unsigned pack2(float lo, float hi) {
  bf16x2 v; v[0] = (__bf16)lo; v[1] = (__bf16)hi; return __builtin_bit_cast(unsigned, v);
}
template <int N> struct IC { static constexpr int v = N; };
template <int B, int E, class Fn> __device__ __forceinline__ void static_for(Fn&& f) {
  if constexpr (B < E) { f(IC<B>{}); static_for<B + 1, E>(f); }
}

__device__ __forceinline__ void dma16(const void* sbase, unsigned voff, unsigned lds_dst) {
  unsigned keep;
  asm volatile("s_mov_b32 %0, m0\n\ts_mov_b32 m0, %3\n\ts_nop 0\n\tglobal_load_lds_dwordx4 %1, %2\n\ts_mov_b32 m0, %0"
               : "=&s"(keep) : "v"(voff), "s"(sbase), "s"(lds_dst) : "memory");
}
__device__ __forceinline__ void dma16_sc1(const void* sbase, unsigned voff, unsigned lds_dst) {
  unsigned keep;
  asm volatile("s_mov_b32 %0, m0\n\ts_mov_b32 m0, %3\n\ts_nop 0\n\tglobal_load_lds_dwordx4 %1, %2 sc1\n\ts_mov_b32 m0, %0"
               : "=&s"(keep) : "v"(voff), "s"(sbase), "s"(lds_dst) : "memory");
}
__device__ __forceinline__ void gload16(u32x4& dst, const void* sbase, unsigned voff) {
  asm volatile("global_load_dwordx4 %0, %1, %2" : "=v"(dst) : "v"(voff), "s"(sbase));
}
__device__ __forceinline__ void gload16_sc1(u32x4& dst, const void* sbase, unsigned voff) {
  asm volatile("global_load_dwordx4 %0, %1, %2 sc1" : "=v"(dst) : "v"(voff), "s"(sbase));
}
__device__ __forceinline__ void gload8(u32x2& dst, const void* sbase, unsigned voff) {
  asm volatile("global_load_dwordx2 %0, %1, %2" : "=v"(dst) : "v"(voff), "s"(sbase));
}
__device__ __forceinline__ void gload8_sc1(u32x2& dst, const void* sbase, unsigned voff) {
  asm volatile("global_load_dwordx2 %0, %1, %2 sc1" : "=v"(dst) : "v"(voff), "s"(sbase));
}
__device__ __forceinline__ void gload4_sc1(unsigned& dst, const void* sbase, unsigned voff) {
  asm volatile("global_load_dword %0, %1, %2 sc1" : "=v"(dst) : "v"(voff), "s"(sbase));
}
__device__ __forceinline__ void gstore16_sc1(void* sbase, unsigned voff, u32x4 v) {
  asm volatile("global_store_dwordx4 %0, %1, %2 sc1\n\ts_nop 1" ::"v"(voff), "v"(v), "s"(sbase) : "memory");
}
__device__ __forceinline__ void gstore8_sc1(void* sbase, unsigned voff, u32x2 v) {
  asm volatile("global_store_dwordx2 %0, %1, %2 sc1" ::"v"(voff), "v"(v), "s"(sbase) : "memory");
}
__device__ __forceinline__ void gstore8(void* sbase, unsigned voff, u32x2 v) {
  asm volatile("global_store_dwordx2 %0, %1, %2" ::"v"(voff), "v"(v), "s"(sbase) : "memory");
}
__device__ __forceinline__ void gload4(unsigned& dst, const void* sbase, unsigned voff) {
  asm volatile("global_load_dword %0, %1, %2" : "=v"(dst) : "v"(voff), "s"(sbase));
}
__device__ __forceinline__ void touch(unsigned& d, const void* sbase, unsigned voff) {
  asm volatile("global_load_dword %0, %1, %2" : "+v"(d) : "v"(voff), "s"(sbase));
}
__device__ __forceinline__ void touch_sc1(unsigned& d, const void* sbase, unsigned voff) {
  asm volatile("global_load_dword %0, %1, %2 sc1" : "+v"(d) : "v"(voff), "s"(sbase));
}
template <class T> __device__ __forceinline__ void pin(T& v) { asm volatile("" : "+v"(v)); }
#define VMWAIT(n) asm volatile("s_waitcnt vmcnt(%0)" ::"n"(n) : "memory")
#define BAR() __builtin_amdgcn_s_barrier()
#define LBAR() do { asm volatile("s_waitcnt lgkmcnt(0)" ::: "memory"); __builtin_amdgcn_s_barrier(); } while (0)
#define SCHED() __builtin_amdgcn_sched_barrier(0)
template <class TA, class TB> __device__ __forceinline__ void MFMA0(f32x4& d, const TA& a, const TB& b) {
  asm volatile("v_mfma_f32_16x16x32_bf16 %0, %1, %2, 0" : "=&v"(d) : "v"(a), "v"(b));
}
template <class TA, class TB> __device__ __forceinline__ void MFMA(f32x4& d, const TA& a, const TB& b) {
  asm volatile("v_mfma_f32_16x16x32_bf16 %0, %1, %2, %0" : "+v"(d) : "v"(a), "v"(b));
}

__device__ __forceinline__ void set_flag(unsigned* f, unsigned tag) {
  __hip_atomic_store((GASP(unsigned))f, tag, __ATOMIC_RELAXED, __HIP_MEMORY_SCOPE_AGENT);
}
__device__ __forceinline__ void wait_flags(const unsigned* ctl, int widx, unsigned tag) {
#pragma nounroll
  for (int spins = 0; spins < 40000; ++spins) {
    const unsigned v = __hip_atomic_load((GASP(const unsigned))(ctl + widx), __ATOMIC_RELAXED, __HIP_MEMORY_SCOPE_AGENT);
    if (__all(v == tag)) break;
    __builtin_amdgcn_s_sleep(1);
  }
  asm volatile("" ::: "memory");
}
__device__ __forceinline__ void wave_sum2(float& a, float& b) {
#define DPPADD(v, ctrl, rm) v += __int_as_float(__builtin_amdgcn_update_dpp(0, __float_as_int(v), ctrl, rm, 0xf, false))
#define DPP2(ctrl, rm) DPPADD(a, ctrl, rm); DPPADD(b, ctrl, rm)
  DPP2(0x111, 0xf); DPP2(0x112, 0xf); DPP2(0x114, 0xf); DPP2(0x118, 0xf); DPP2(0x142, 0xa); DPP2(0x143, 0xc);
#undef DPP2
#undef DPPADD
  a = __int_as_float(__builtin_amdgcn_readlane(__float_as_int(a), 63));
  b = __int_as_float(__builtin_amdgcn_readlane(__float_as_int(b), 63));
}

constexpr int SLOT = 8192, PD = 3, NQ = 4;
struct Op { int kind, a, b; };
template <int NSTEP> struct Sched {
  int ks[NSTEP], tile[NSTEP], gu[NSTEP];
  Op op[NSTEP];
  int sync[NSTEP];
  int alo[NSTEP], ahi[NSTEP];
  int ev[NSTEP], eslot[NSTEP];
  int foff[NSTEP], bid[NSTEP], pwin[NSTEP];
  int slot[5 * 32];
  int npd; Op prd[40];
  int npa; Op pra[16];
  int first_wait;
  int nrest; int rest[40];
  int nst; Op strest[12];
  int xbar, cbar, hbar, h2bar, pbar, zbar, after_side, h1c, h1wait, cvlast;
  int err;
};
constexpr int NCV = 84;
struct Cfg { int NG, K1, AW, NS, KPRE, KA, PRE0, GAP, ECAD, pairs, NPRE, AW0; };
template <int NSTEP> constexpr Sched<NSTEP> make_sched(const Cfg c) {
  Sched<NSTEP> s{};
  int n = 0;
  bool isbar[NSTEP + 1] = {};
  auto add = [&](int tl, int k) { for (int g = 0; g < c.NG; ++g) { s.ks[n] = k; s.tile[n] = tl; s.gu[n] = g; s.op[n] = {0, 0, 0}; s.sync[n] = -2; s.ev[n] = -1; ++n; } };
  for (int i = 0; i < c.NPRE; ++i) add(-1, -1);
  for (int k = 0; k < c.K1; ++k) { if (n >= PD) isbar[n - PD] = true; for (int tl = 0; tl < 9; ++tl) add(tl, k); }
  const int ngrp = c.pairs ? 5 : 3, gsz = c.pairs ? 2 : 4;
  for (int gi = 0; gi < ngrp; ++gi)
    for (int k = c.K1; k < 18; ++k) {
      const int per = (c.pairs || gi == ngrp - 1) ? 4 : 2;
      if ((k - c.K1) % per == 0) isbar[n - PD] = true;
      if (gi == ngrp - 1) add(8, k); else for (int j = 0; j < gsz; ++j) add(gi * gsz + j, k);
    }
  int fu[5 * 32] = {}, lu[5 * 32] = {};
  for (int i = 0; i < 5 * 32; ++i) { fu[i] = -1; lu[i] = -1; }
  for (int i = 0; i < NSTEP; ++i) {
    const int tl = s.tile[i], k = s.ks[i];
    if (tl < 0) continue;
    const int idb = tl < 4 ? 32 + k : (tl < 8 ? 96 + k : 128 + k / 4), ida = 64 + k;
    if (fu[idb] < 0) fu[idb] = i;
    lu[idb] = i;
    if (fu[ida] < 0) fu[ida] = i;
    lu[ida] = i;
  }
  int fin[9] = {};
  for (int i = 0; i < NSTEP; ++i) if (s.ks[i] == 17) fin[s.tile[i]] = i + 1;
  if (c.pairs) isbar[fin[3] + 1] = true;
  Op ops[128] = {}; int dl[128] = {}; int no = 0;
  auto pubbar = [&](int need) { int p = need; while (p >= 0 && !isbar[p]) --p; return p; };
  auto nextbar = [&](int after) { int p = after + 1; while (p < NSTEP && !isbar[p]) ++p; return p; };
  for (int k = 0; k < 18; ++k) {
    for (int m = 0; m < 2; ++m) { ops[no] = {2, k, m}; dl[no] = pubbar(fu[64 + k] - PD); ++no; }
    ops[no] = {1, k, 0}; dl[no] = pubbar(fu[32 + k] - PD); ++no;
    ops[no] = {3, k, 0}; dl[no] = pubbar(fu[96 + k] - PD); ++no;
    if (k % 4 == 0) { ops[no] = {4, k / 4, 0}; dl[no] = pubbar(fu[128 + k / 4] - PD); ++no; }
  }
  const int nload = no;
  const int tfree = c.pairs ? fin[3] + 1 : NSTEP + 1;
  int posn[128] = {};
  int freeat[24] = {};
  auto place = [&](int pos, Op o) { while (pos < NSTEP && s.op[pos].kind != 0) ++pos; if (pos < NSTEP) s.op[pos] = o; else s.strest[s.nst++] = o; return pos; };
  auto side = [&](int pos, Op o, int deadline) { const int p = place(pos, o); if (p >= NSTEP || p >= deadline) s.err = 6; ops[no] = o; dl[no] = deadline; posn[no] = p; ++no; return p; };
  int cvend = 0;
  s.xbar = s.cbar = s.hbar = s.h2bar = s.pbar = s.zbar = -1;
  if (!c.pairs) {
    s.cvlast = NCV - c.NPRE;
    const int e1 = nextbar(s.cvlast);
    for (int j = 0; j < 10; ++j) s.ev[e1 + j] = 20 + j;
    s.pbar = nextbar(e1 + 10);
    for (int j = 0; j < 6; ++j) s.ev[s.pbar + j] = 30 + j;
    cvend = nextbar(s.pbar + 6);
    for (int sl = 10; sl < 16; ++sl) freeat[sl] = cvend;
    s.zbar = nextbar(cvend + 4);
    side(s.pbar + 3, Op{13, 0, 0}, s.zbar); side(s.pbar + 4, Op{13, 1, 0}, s.zbar); side(s.pbar + 6, Op{13, 2, 0}, s.zbar);
    s.h2bar = nextbar(c.NPRE);
    s.hbar = nextbar(nextbar(nextbar(nextbar(s.h2bar))));
    side(s.hbar - 14, Op{12, 1, 0}, s.hbar);
    s.h1c = 5;
    side(0, Op{12, 0, 0}, NSTEP + 9);
    s.eslot[0] = e1;
  }
  int npre = 0;
  for (int i = 0; i < nload; ++i) {
    const int kk = ops[i].kind == 4 ? ops[i].a * 4 : ops[i].a;
    posn[i] = -3;
    if (ops[i].kind != 2 && kk < c.KPRE) {
      const int sl = c.PRE0 + npre++;
      if (sl >= c.NS) s.err = 4;
      posn[i] = -2; ops[i].b = sl; s.slot[ops[i].kind * 32 + ops[i].a] = sl; freeat[sl] = nextbar(lu[ops[i].kind * 32 + ops[i].a]);
      s.prd[s.npd++] = ops[i];
    } else if (ops[i].kind == 2 && kk < c.KA) {
      posn[i] = -1; s.pra[s.npa++] = ops[i]; if (dl[i] < 0) s.first_wait = s.npa;
    } else if (dl[i] < 0) s.err = 2;
  }
  int prev = -1;
  for (int pos = 0; pos < NSTEP; ++pos) {
    if (s.op[pos].kind != 0) continue;
    int flo = -1, fany = -1;
    for (int sl = 0; sl < c.NS; ++sl) if (freeat[sl] <= pos) { fany = sl; if (sl < 12 && flo < 0) flo = sl; }
    const int aw = pos < s.cvlast ? c.AW0 : (pos < s.pbar ? c.AW0 + 4 : c.AW);
    int best = -1, bs = -1;
    for (int i = 0; i < nload; ++i) {
      if (posn[i] != -3) continue;
      int sl = 0;
      if (ops[i].kind == 2) {
        if (!(ops[i].a < aw || lu[64 + ops[i].a - aw] < pos)) continue;
        if ((ops[i].a >= 8 && pos < s.hbar) || pos < s.h1c) continue;
      } else { sl = lu[ops[i].kind * 32 + ops[i].a] >= tfree ? flo : fany; if (sl < 0) continue; }
      if (best < 0 || dl[i] < dl[best]) { best = i; bs = sl; }
    }
    if (best < 0) continue;
    if (pos - prev < ((dl[best] - pos < 24 * c.NG) ? 1 : c.GAP)) continue;
    if (pos >= dl[best]) s.err = 1;
    if (ops[best].kind != 2) { ops[best].b = bs; s.slot[ops[best].kind * 32 + ops[best].a] = bs; freeat[bs] = nextbar(lu[ops[best].kind * 32 + ops[best].a]); }
    s.op[pos] = ops[best]; posn[best] = pos; prev = pos;
  }
  int lastload = 0;
  for (int i = 0; i < nload; ++i) { if (posn[i] == -3) s.err = 5; if (posn[i] > lastload) lastload = posn[i]; }
  int lastside = 0;
  if (c.pairs) {
    int order[36] = {}; int ne = 0;
    for (int gi = 0; gi < 5; ++gi) {
      if (gi == 4) { for (int j = 0; j < 4; ++j) order[ne++] = 8 * 4 + j; }
      else for (int j = 0; j < 8; ++j) order[ne++] = (2 * gi + ((j >> 1) & 1)) * 4 + (j >> 2) * 2 + (j & 1);
    }
    int done_at[36] = {};
    int cur = 0, nxt = 0;
    for (; cur < 36; ++cur) {
      int at = fin[order[cur] >> 2] + 3; if (at < nxt) at = nxt;
      if (at + 2 >= NSTEP) break;
      s.ev[at] = order[cur]; s.eslot[at] = cur % 3; done_at[cur] = at + 3; nxt = at + c.ECAD;
    }
    for (int i = cur; i < 36; ++i) { s.rest[s.nrest++] = order[i]; done_at[i] = NSTEP + 1; }
    s.hbar = nextbar(done_at[15] + 20);
    for (int i = 0; i < 36; ++i) {
      const int hu = order[i], t = hu >> 2, m = (hu >> 1) & 1, hh = hu & 1;
      if (hh != 1 || (t != 8 && (t & 1) == 0)) continue;
      if (t < 4) side(done_at[i], Op{5, t >> 1, m}, s.hbar);
      else place(done_at[i], t == 8 ? Op{6, 0, m} : Op{5, t >> 1, m});
    }
    s.cbar = tfree;
    side(18 * c.K1 + 16, Op{7, 0, 0}, tfree);
    side(18 * c.K1 + 19, Op{8, 0, 0}, tfree);
    for (int j = 0; j < 8; ++j) lastside = side(tfree + 1 + 3 * j, Op{9, j, 0}, NSTEP + 9);
  } else {
    const int f = fin[3] + 3;
    if (f < cvend) s.err = 7;
    for (int j = 0; j < 4; ++j) s.ev[f + j] = j;
    const int bq = nextbar(f + 4);
    if (bq + 12 >= NSTEP) s.err = 3;
    for (int j = 0; j < 4; ++j) { s.ev[bq + 1 + j] = 4 + j; s.ev[bq + 5 + j] = 8 + j; }
    for (int j = 1; j < 4; ++j) place(bq + 7 + j, Op{5, j, 0});
    s.xbar = bq;
    s.cbar = pubbar(NSTEP - 24);
    if (s.cbar <= s.zbar + 24) s.err = 8;
    side(s.cbar - 15, Op{8, 0, 0}, s.cbar);
    side(s.cbar - 14, Op{8, 1, 0}, s.cbar);
    for (int j = 0; j < 6; ++j) side(lastload + 1 + j, Op{9, (j >> 1) * 6 + (j & 1), 0}, NSTEP + 9);
    for (int j = 0; j < 12; ++j) side(s.cbar + 1 + j, Op{9, (j >> 2) * 6 + 2 + (j & 3), 0}, NSTEP + 9);
    for (int j = 0; j < 3; ++j) lastside = side(NSTEP - 4 + j, Op{10, j, 0}, NSTEP + 9);
  }
  int cum[NSTEP + 1] = {}; cum[0] = s.npa;
  for (int i = 0; i < NSTEP; ++i) cum[i + 1] = cum[i] + (s.op[i].kind != 0);
  s.after_side = cum[NSTEP] - cum[lastside] - 1;
  s.h1wait = cum[s.h1c] - 1;
  for (int p = 0; p < NSTEP; ++p) {
    if (!isbar[p]) continue;
    int idx = -1, alo = 99, ahi = -1, pi = 0;
    for (int i = 0; i < no; ++i) {
      if (posn[i] == -2) continue;
      const int my = posn[i] < 0 ? pi++ : cum[posn[i]];
      if (dl[i] == p) { if (my > idx) idx = my; if (ops[i].kind == 2) { if (ops[i].a < alo) alo = ops[i].a; if (ops[i].a + 1 > ahi) ahi = ops[i].a + 1; } }
    }
    s.sync[p] = idx >= 0 ? cum[p] - idx - 1 : ((p <= lastload || p == s.xbar || p == s.cbar || p == s.pbar) ? -1 : -2);
    s.alo[p] = alo; s.ahi[p] = ahi;
  }
  if (s.h2bar >= 0 && s.sync[s.h2bar] < 0) s.err = 9;
  int lastw[5] = {};
  for (int i = 0; i < NSTEP; ++i) {
    const int tl = s.tile[i], k = s.ks[i];
    if (tl < 0) continue;
    s.foff[i] = tl < 8 ? s.slot[(tl < 4 ? 32 : 96) + k] * SLOT + s.gu[i] * 4096
                       : s.slot[128 + k / 4] * SLOT + ((k % 4) * 2 + s.gu[i]) * 1024;
    s.bid[i] = tl < 8 ? (tl & 3) : 4;
    s.pwin[i] = lastw[s.bid[i]]; lastw[s.bid[i]] = s.foff[i] >> 16;
  }
  return s;
}
struct Cv { int part, a, b, acc, first, newa, newb; };
struct CvList { Cv p[NCV]; };
constexpr CvList make_cv() {
  CvList L{}; int n = 0;
  for (int a = 0; a < 8; ++a) {
    L.p[n++] = {0, a, a, 9, a == 0, 1, 1};
    if (a < 4) L.p[n++] = {0, a, 8 + a, 8, a == 0, 0, 1};
  }
  for (int r = 1; r < 3; ++r)
    for (int a = 0; a < 8; ++a)
      for (int bb = 0; bb < 8 - a; ++bb) L.p[n++] = {r, a, bb, a + bb, r == 1 && a == 0, bb == 0, a == 0};
  return L;
}
constexpr CvList CV = make_cv();
#define ROT1 (((idx >> 2) * 2 + (idx & 3) * 4) % 15)
#define ROT2 cb2
#define G1_K1 10
#define G2_K1 8
#define G2_GAP 2
#define G1_KPRE 3
#define G2_KA 4
constexpr Cfg C1 = {2, G1_K1, 18 - G1_K1, 20, G1_KPRE, 2, 5, 3, 4, 1, 0, 0};
#define G2_NPRE 16
#define G2_AW0 6
constexpr int NST2 = 162 + G2_NPRE;
constexpr Cfg C2 = {1, G2_K1, 18 - G2_K1 + 2, 16, 0, 0, 0, G2_GAP, 0, 0, G2_NPRE, G2_AW0};
constexpr Sched<324> SC1 = make_sched<324>(C1);
constexpr Sched<NST2> SC2 = make_sched<NST2>(C2);
static_assert(SC1.err == 0 && SC2.err == 0, "schedule");
constexpr int G2_XOFF = 16 * SLOT;
constexpr int P0_LN = 0, P0_KV = 10240, P0_K = 20480;
constexpr int CS = 4160, HB = 4 * CS, CONV_CH = HB + 4608;
constexpr int CONV0 = 12 * SLOT, CONVZ = CONV0 + 3 * CONV_CH;
constexpr int CONVP = 12 * SLOT, CONVQ = 10 * SLOT;
static_assert(CONVZ + 1024 <= 163840, "conv lds");

__device__ __forceinline__ float silu_mul(float g, float u) {
  return g * __builtin_amdgcn_rcpf(1.0f + __builtin_amdgcn_exp2f(g * -1.4426950408889634f)) * u;
}
__device__ __forceinline__ bf16x8 ld2x8(const char* p) {
  const u32x2 a = *(const u32x2*)p, b = *(const u32x2*)(p + 8);
  return __builtin_bit_cast(bf16x8, (u32x4){a[0], a[1], b[0], b[1]});
}

__global__ __launch_bounds__(512, 2) void k_main(const u16* __restrict__ x, const u16* __restrict__ kk,
                                                 const u16* __restrict__ vv, const u16* __restrict__ sk,
                                                 const u16* __restrict__ hk, const u16* __restrict__ Dp,
                                                 const u16* __restrict__ w1, const u16* __restrict__ w3,
                                                 const u16* __restrict__ w2, char* __restrict__ ws,
                                                 u16* __restrict__ out) {
  __shared__ __attribute__((aligned(16))) char sm[163840];
  const unsigned lbase = (unsigned)(uintptr_t)(LDSP(char))sm;
  const int tid = threadIdx.x, l = tid & 63, b = blockIdx.x;
  const int w = __builtin_amdgcn_readfirstlane(tid >> 6);
  const int xcd = b & 7, idx = b >> 3;
  unsigned* ctl = (unsigned*)(ws + WS_CTL);
  const unsigned aoff = l * 16;
  const int g = l >> 4, q = (l >> 2) & 3, p = l & 3, trow = 4 * g + q;
  const int drow = 8 * (w & 3) + (l >> 3);
  const unsigned dsw = ((l & 7) ^ (((drow >> 1) & 3) << 1)) << 4;
  ST_DECL();
  STAMP(0);
  unsigned tagv;
  gload4_sc1(tagv, ctl, 0u);

  const int rbk = 4 * (xcd >> 2) + (idx & 3), cb = 8 * (xcd & 3) + (idx >> 2);
  const char* csrc1 = (const char*)((w >> 2) ? w3 : w1) + cb * 256;
  const char* lsrc1 = (const char*)((w & 1) ? w3 : w1) + (4096 + 16 * cb) * 2;
  const int rot1 = ROT1;
  auto kr1 = [&](int k) { const int e = k + rot1; return k < 3 ? k : (e < 18 ? e : e - 15); };
  auto g1_dma = [&](auto K, auto Aa, auto Sl) {
    constexpr int kind = K.v, a = Aa.v, sl = Sl.v;
    if constexpr (kind == 1) dma16(csrc1 + (size_t)kr1(a) * 32 * (F_ * 2), drow * (F_ * 2) + dsw, lbase + w * 1024 + sl * SLOT);
    if constexpr (kind == 3) dma16(csrc1 + 128 + (size_t)kr1(a) * 32 * (F_ * 2), drow * (F_ * 2) + dsw, lbase + w * 1024 + sl * SLOT);
    if constexpr (kind == 4) {
      const int wq = a < 4 ? w : (w & 3);
      dma16(lsrc1 + (size_t)kr1(4 * a + (wq >> 1)) * 32 * (F_ * 2), (unsigned)(l >> 1) * (F_ * 2) + (l & 1) * 16,
            lbase + sl * SLOT + wq * 1024);
    }
  };
  const int sp = xcd & 3, cb2 = idx & 3, rbl = idx >> 2, rb2 = 8 * (xcd >> 2) + rbl;
  const int mw = w & 3, kh = w >> 2;
  auto kent = [&](int half, int k) {
    return k < 16 ? 32 * sp + 16 * half + 4 * ((((k & 7) >> 1) + ROT2) & 3) + (k & 1) + 2 * (k >> 3) : 112 + 4 * sp + 2 * half + k;
  };
  auto g2_dma = [&](auto K, auto Aa, auto Sl) {
    constexpr int kind = K.v, a = Aa.v, sl = Sl.v;
    const char* csrc = (const char*)w2 + cb2 * 256;
    if constexpr (kind == 1) dma16(csrc + (size_t)kent(kh, a) * 32 * (D_ * 2), drow * (D_ * 2) + dsw, lbase + w * 1024 + sl * SLOT);
    if constexpr (kind == 3) dma16(csrc + 128 + (size_t)kent(kh, a) * 32 * (D_ * 2), drow * (D_ * 2) + dsw, lbase + w * 1024 + sl * SLOT);
    if constexpr (kind == 4) {
      const int wq = a < 4 ? w : (w & 3);
      dma16((const char*)w2 + 1024 + cb2 * 32 + (size_t)kent(wq & 1, 4 * a + (wq >> 1)) * 32 * (D_ * 2),
            (unsigned)(l >> 1) * (D_ * 2) + (l & 1) * 16, lbase + sl * SLOT + wq * 1024);
    }
  };

  unsigned tag;
  {
    u32x4 xa, skv;
    unsigned xe;
    SUB(1, 0);
    const char* xrow = (const char*)x + (size_t)(8 * b + w) * (D_ * 2);
    gload16(xa, xrow, aoff);
    gload4(xe, xrow + 1024, (l & 31) * 4);
    const int tbk = b >> 3, cbk = b & 7, t0 = 64 * tbk;
    const int kt = tid < 396 ? tid : 0, cp = kt % 36, tg = kt / 36;
    BAR();
#pragma unroll
    for (int j = 0; j < 3; ++j) {
      int pi = j * 7 + (w < 7 ? w : 0); if (pi >= 20) pi -= 1;
      const int isv = pi >= 10, ci = (pi - 10 * isv) * 64 + l, row = ci / 9;
      int tt = t0 - (isv ? 2 : 5) + row; tt = tt < 0 ? 0 : (tt > S_ - 1 ? S_ - 1 : tt);
      dma16(isv ? vv : kk, (unsigned)(tt * D_ + 72 * cbk + (ci - row * 9) * 8) * 2, lbase + P0_K + pi * 1024);
    }
    gload16(skv, sk, (unsigned)(72 * cbk + 2 * cp) * 8);
    constexpr int NPD = SC1.npd, NB1 = 5;
    VMWAIT(4);
    SUB(1, 1);
    pin(xa); pin(xe); pin(tagv);
    {
      float v[10];
#pragma unroll
      for (int j = 0; j < 4; ++j) { v[2 * j] = lo2f(xa[j]); v[2 * j + 1] = hi2f(xa[j]); }
      v[8] = lo2f(xe); v[9] = hi2f(xe);
      const float pv = __int_as_float(__builtin_amdgcn_readfirstlane(__float_as_int(v[0])));
      float d[10];
#pragma unroll
      for (int j = 0; j < 10; ++j) d[j] = v[j] - pv;
      if (l >= 32) { d[8] = 0.f; d[9] = 0.f; }
      float s1 = ((d[0] + d[1]) + (d[2] + d[3])) + ((d[4] + d[5]) + (d[6] + d[7])) + (d[8] + d[9]);
      float qa = d[0] * d[0], qb = d[1] * d[1];
#pragma unroll
      for (int j = 2; j < 10; j += 2) { qa = __builtin_fmaf(d[j], d[j], qa); qb = __builtin_fmaf(d[j + 1], d[j + 1], qb); }
      float s2 = qa + qb;
      wave_sum2(s1, s2);
      const float m1 = s1 * (1.0f / D_), var = __builtin_fmaxf(__builtin_fmaf(-m1, m1, s2 * (1.0f / D_)), 0.f);
      const float mb = rb(pv + m1), sd = rb(__builtin_amdgcn_sqrtf(rb(rb(var) + 1e-5f))), rs = __builtin_amdgcn_rcpf(sd);
      unsigned o[5];
#pragma unroll
      for (int j = 0; j < 5; ++j) {
        const unsigned dd = pack2(v[2 * j] - mb, v[2 * j + 1] - mb);
        const float d0 = lo2f(dd), d1 = hi2f(dd);
        float q0 = d0 * rs, q1 = d1 * rs;
        q0 = __builtin_fmaf(__builtin_fmaf(-q0, sd, d0), rs, q0);
        q1 = __builtin_fmaf(__builtin_fmaf(-q1, sd, d1), rs, q1);
        o[j] = pack2(q0, q1);
      }
      const int cq = l & 3;
      char* d0 = sm + P0_LN + (l >> 2) * 512 + (2 * (cq & 1)) * 128 + w * 16 + (cq >> 1) * 8;
      *(u32x2*)d0 = (u32x2){o[0], o[1]};
      *(u32x2*)(d0 + 128) = (u32x2){o[2], o[3]};
      if (l < 32) *(unsigned*)(sm + P0_LN + 8192 + (l >> 4) * 512 + ((l >> 1) & 3) * 128 + w * 16 + ((l >> 3) & 1) * 8 + (l & 1) * 4) = o[4];
    }
    tag = __builtin_amdgcn_readfirstlane(tagv) + 1u;
    LBAR();
    SUB(1, 2);
    if (w == 7) {
      char* xdst = ws + WS_XNF + (size_t)(b >> 1) * 18432;
      u32x4 pc[9];
#pragma unroll
      for (int i = 0; i < 9; ++i) pc[i] = *(const u32x4*)(sm + P0_LN + i * 1024 + aoff);
#pragma unroll
      for (int i = 0; i < 9; ++i) gstore16_sc1(xdst + i * 2048, (unsigned)(l >> 3) * 256 + (l & 7) * 16 + (b & 1) * 128, pc[i]);
      BAR();
      VMWAIT(0);
      if (l == 0) set_flag(ctl + FL_X + b, tag);
    }
    static_for<0, NB1>([&](auto I) { constexpr Op o = SC1.prd[I.v]; g1_dma(IC<o.kind>{}, IC<o.a>{}, IC<o.b>{}); });
    VMWAIT(NB1);
    pin(skv);
    if (w != 7) BAR();
    SUB(1, 3);
    if (tid < 396) {
      const unsigned* kp = (const unsigned*)(sm + P0_K) + (6 * tg) * 36 + cp;
      const unsigned* vp = (const unsigned*)(sm + P0_K + 10240) + (6 * tg) * 36 + cp;
      unsigned kd[9], vd[6];
#pragma unroll
      for (int i = 0; i < 9; ++i) kd[i] = kp[i * 36];
#pragma unroll
      for (int i = 0; i < 6; ++i) vd[i] = vp[i * 36];
      if (tbk == 0 && tg == 0) {
#pragma unroll
        for (int i = 0; i < 5; ++i) kd[i] = 0u;
        vd[0] = 0u; vd[1] = 0u;
      }
      unsigned* T = (unsigned*)(sm + P0_KV) + (2 * cp) * 33 + 32 - 3 * tg;
#pragma unroll
      for (int e = 0; e < 2; ++e) {
        const float c0 = lo2f(skv[2 * e]), c1 = hi2f(skv[2 * e]), c2 = lo2f(skv[2 * e + 1]), c3 = hi2f(skv[2 * e + 1]);
        float kf[9], r[6];
#pragma unroll
        for (int i = 0; i < 9; ++i) kf[i] = e ? hi2f(kd[i]) : lo2f(kd[i]);
#pragma unroll
        for (int u = 0; u < 6; ++u) {
          const float a = __builtin_fmaf(kf[u + 3], c0, __builtin_fmaf(kf[u + 2], c1, __builtin_fmaf(kf[u + 1], c2, kf[u] * c3)));
          r[u] = rb(a) * (e ? hi2f(vd[u]) : lo2f(vd[u]));
        }
#pragma unroll
        for (int u = 0; u < 3; ++u) T[e * 33 - u] = pack2(r[2 * u + 1], r[2 * u]);
      }
    }
    SUB(1, 4);
    LBAR();
    SUB(1, 5);
    {
      const unsigned* T = (const unsigned*)(sm + P0_KV);
      const int u0 = 1984 - t0;
      char* r0 = ws + WS_R0 + (size_t)(72 * cbk) * (RP * 2) + u0 * 2;
      char* r1 = ws + WS_R1 + (size_t)(72 * cbk) * (RP * 2) + u0 * 2;
#pragma nounroll
      for (int id = tid; id < 576; id += 512) {
        const int r = id >> 3, ch = id & 7;
        unsigned d[5];
#pragma unroll
        for (int j = 0; j < 5; ++j) d[j] = T[r * 33 + ch * 4 + j];
        gstore16_sc1(r0, (unsigned)r * (RP * 2) + ch * 16, (u32x4){d[0], d[1], d[2], d[3]});
        gstore16_sc1(r1, (unsigned)r * (RP * 2) + ch * 16,
                     (u32x4){__builtin_amdgcn_alignbyte(d[1], d[0], 2), __builtin_amdgcn_alignbyte(d[2], d[1], 2),
                             __builtin_amdgcn_alignbyte(d[3], d[2], 2), __builtin_amdgcn_alignbyte(d[4], d[3], 2)});
      }
      if (tbk == 0 && tid < 72) {
        gstore16_sc1(r0, (unsigned)tid * (RP * 2) + 128, (u32x4){0u, 0u, 0u, 0u});
        gstore16_sc1(r1, (unsigned)tid * (RP * 2) + 128, (u32x4){0u, 0u, 0u, 0u});
      }
    }
    SUB(1, 6);
    static_for<NB1, NPD>([&](auto I) { constexpr Op o = SC1.prd[I.v]; g1_dma(IC<o.kind>{}, IC<o.a>{}, IC<o.b>{}); });
    SUB(1, 7);
  }
  STAMP(1);

  const int hslot = xcd * 32 + (idx & 3) * 8 + (idx >> 2);
  const int kvw = FL_KV + (w < 4 ? ((b + 256 * (l >> 5)) / 72) : 7) * 32 + (l & 31);
  {
#define SC SC1
    constexpr int NSTEP = 324;
    const int mt0 = 16 * rbk + 2 * w;
    wait_flags(ctl, FL_X + 2 * mt0 + (l & 3), tag);
    STAMP(2);
    const char* asrc = ws + WS_XNF + (size_t)mt0 * 18432;
    char* hdst = ws + WS_HF + ((size_t)mt0 * 144 + 4 * cb) * 1024;
    char* ldst = ws + WS_HF + ((size_t)mt0 * 144 + 128 + (cb >> 1)) * 1024 + (cb & 1) * 8;
    u32x4 A[18][2];
    unsigned pk[9][2][2];
    unsigned fl0 = 0, fl1;
    auto do_op = [&](auto K, auto Aa, auto Bb) {
      constexpr int kind = K.v, a = Aa.v, bb = Bb.v;
      if constexpr (kind == 1 || kind == 3 || kind == 4) g1_dma(K, Aa, Bb);
      if constexpr (kind == 2) gload16_sc1(A[a][bb], asrc + kr1(a) * 1024 + bb * 18432, aoff);
      if constexpr (kind == 5) gstore16_sc1(hdst + (bb * 144 + a) * 1024, aoff, (u32x4){pk[2 * a][bb][0], pk[2 * a][bb][1], pk[2 * a + 1][bb][0], pk[2 * a + 1][bb][1]});
      if constexpr (kind == 6) gstore8_sc1(ldst + bb * 144 * 1024, aoff, (u32x2){pk[8][bb][0], pk[8][bb][1]});
      if constexpr (kind == 7) {
        const int ln = idx * 324 + (tid < 324 ? tid : 0), row = ln / 9;
        touch(fl0, w2, (unsigned)((row < 1024 ? 1024 * sp + row : 3072 + 128 * sp + row) * 9 + (ln - row * 9)) * 128);
      }
      if constexpr (kind == 8) gload4_sc1(fl1, ctl, (unsigned)kvw * 4);
      if constexpr (kind == 9) {
        if constexpr (a < 6) {
          const int c = a < 4 ? b + 256 * (a >> 1) : 512 + (b >> 2), sg = 3 - (w >> 1), part = (w & 1) * 2 + (a & 1);
          dma16_sc1(ws + ((sg & 1) ? WS_R1 : WS_R0) + (size_t)c * (RP * 2) + (sg >> 1) * 4 + part * 1024, aoff,
                    lbase + CONV0 + (a >> 1) * CONV_CH + (w >> 1) * CS + part * 1024);
        } else {
          const int ci = a == 6 ? (w >> 2) : 2, c = a == 6 ? b + 256 * (w >> 2) : 512 + (b >> 2);
          dma16((const char*)hk + (size_t)c * (S_ * 2) + (w & 3) * 1024, aoff, lbase + CONV0 + ci * CONV_CH + HB + 480 + (w & 3) * 1024);
        }
      }
    };
    static_for<0, SC.npa>([&](auto I) { constexpr Op o = SC.pra[I.v]; do_op(IC<o.kind>{}, IC<o.a>{}, IC<o.b>{}); });
    unsigned tb[5];
#pragma unroll
    for (int t = 0; t < 4; ++t) tb[t] = lbase + trow * 128 + ((t ^ ((trow >> 1) & 3)) << 5) + 8 * p;
    tb[4] = lbase + trow * 32 + 8 * p;
    f32x4 acc[9][2][2];
    s16x4 blo[NQ], bhi[NQ];
    auto ld_lo = [&](auto I) {
      constexpr int n = I.v; constexpr int bi = SC.bid[n], fo = SC.foff[n], wn = fo >> 16, pw = SC.pwin[n];
      if constexpr (wn != pw) { tb[bi] += (unsigned)((wn - pw) * 65536); pin(tb[bi]); }
      blo[n % NQ] = __builtin_amdgcn_ds_read_tr16_b64_v4i16((LDSP(s16x4))(tb[bi] + (fo & 65535)));
    };
    auto ld_hi = [&](auto I) {
      constexpr int n = I.v; constexpr int bi = SC.bid[n], fo = SC.foff[n];
      bhi[n % NQ] = __builtin_amdgcn_ds_read_tr16_b64_v4i16((LDSP(s16x4))(tb[bi] + (fo & 65535) + (bi < 4 ? 2048 : 512)));
    };
    float et[3][2];
    auto epi_stage = [&](auto I, auto H) {
      constexpr int n = I.v, slot2 = H.v;
      static_for<0, 3>([&](auto Dd) {
        constexpr int d = Dd.v;
        if constexpr (n - d >= 0) {
          constexpr int ep = SC.ev[n - d];
          if constexpr (ep >= 0) {
            constexpr int t = ep >> 2, m = (ep >> 1) & 1, hh = ep & 1, stg = 2 * d + slot2;
            const float g0 = acc[t][m][0][2 * hh], g1 = acc[t][m][0][2 * hh + 1];
            float& e0 = et[SC.eslot[n - d]][0]; float& e1 = et[SC.eslot[n - d]][1];
            if constexpr (stg == 0) { e0 = g0 * -1.4426950408889634f; pin(e0); e1 = g1 * -1.4426950408889634f; }
            if constexpr (stg == 1) { e0 = __builtin_amdgcn_exp2f(e0); e1 = __builtin_amdgcn_exp2f(e1); }
            if constexpr (stg == 2) { e0 = 1.0f + e0; pin(e0); e1 = 1.0f + e1; }
            if constexpr (stg == 3) { e0 = __builtin_amdgcn_rcpf(e0); e1 = __builtin_amdgcn_rcpf(e1); }
            if constexpr (stg == 4) { e0 *= g0; pin(e0); e1 *= g1; pin(e1); }
            if constexpr (stg == 5) { e0 *= acc[t][m][1][2 * hh]; pin(e0); e1 *= acc[t][m][1][2 * hh + 1]; pk[t][m][hh] = pack2(e0, e1); }
          }
        }
      });
    };
    VMWAIT(SC.npa - SC.first_wait);
    pin(A[0][0]); pin(A[0][1]);
    LBAR();
    if (tid == 0) set_flag(ctl + FL_KV + (b & 7) * 32 + (b >> 3), tag);
    STAMP(3);
    static_for<0, PD>([&](auto I) { ld_lo(I); ld_hi(I); });
    SCHED();
    static_for<0, NSTEP>([&](auto I) {
      constexpr int n = I.v;
      constexpr int ks = SC.ks[n], tile = SC.tile[n], gu = SC.gu[n], sy = SC.sync[n];
      if constexpr (sy >= 0) {
        static_assert(sy < 64, "vmcnt");
        VMWAIT(sy);
        static_for<SC.alo[n], SC.ahi[n]>([&](auto K) { pin(A[K.v][0]); pin(A[K.v][1]); });
      }
      if constexpr (n == SC.cbar) { pin(fl0); pin(fl1); if (!__all(fl1 == tag)) wait_flags(ctl, kvw, tag); }
      if constexpr (sy >= -1) { BAR(); SCHED(); }
      if constexpr (n == SC.hbar) { if (tid == 0) set_flag(ctl + FL_H + hslot, tag); }
      const bf16x8 bfr = __builtin_bit_cast(bf16x8, __builtin_shufflevector(blo[n % NQ], bhi[n % NQ], 0, 1, 2, 3, 4, 5, 6, 7));
      if constexpr (n + PD < NSTEP) ld_lo(IC<n + PD>{});
      epi_stage(I, IC<0>{});
      SCHED();
      if constexpr (ks == 0) MFMA0(acc[tile][0][gu], bfr, A[ks][0]); else MFMA(acc[tile][0][gu], bfr, A[ks][0]);
      SCHED();
      if constexpr (n + PD < NSTEP) ld_hi(IC<n + PD>{});
      constexpr Op op = SC.op[n];
      do_op(IC<op.kind>{}, IC<op.a>{}, IC<op.b>{});
      epi_stage(I, IC<1>{});
      SCHED();
      if constexpr (ks == 0) MFMA0(acc[tile][1][gu], bfr, A[ks][1]); else MFMA(acc[tile][1][gu], bfr, A[ks][1]);
      SCHED();
    });
    STAMP(4);
    asm volatile("s_nop 7\n\ts_nop 7" ::: "memory");
    SCHED();
    static_for<0, SC.nrest>([&](auto I) {
      constexpr int ep = SC.rest[I.v], t = ep >> 2, m = (ep >> 1) & 1, hh = ep & 1;
      pk[t][m][hh] = pack2(silu_mul(acc[t][m][0][2 * hh], acc[t][m][1][2 * hh]), silu_mul(acc[t][m][0][2 * hh + 1], acc[t][m][1][2 * hh + 1]));
    });
    static_for<0, SC.nst>([&](auto I) { constexpr Op o = SC.strest[I.v]; do_op(IC<o.kind>{}, IC<o.a>{}, IC<o.b>{}); });
  }
  STAMP(5);

#undef SC
#define SC SC2
  {
    constexpr int NSTEP = NST2;
    const int mtk = 8 * rb2 + 2 * mw + kh, mtg = 8 * rb2 + 2 * mw + 1 - kh;
    const int qd = b & 3, r = l & 15;
    if (tid < 178) {
      const int o = tid < 24 ? CONV0 + (tid >> 3) * CONV_CH + ((tid >> 1) & 3) * CS + 4096 + (tid & 1) * 16
                             : (tid < 114 ? CONV0 + ((tid - 24) / 30) * CONV_CH + HB + ((tid - 24) % 30) * 16 : CONVZ + (tid - 114) * 16);
      *(u32x4*)(sm + o) = (u32x4){0u, 0u, 0u, 0u};
    }
    unsigned dvr;
    gload4(dvr, Dp, (unsigned)(((l & 1) ? 512 + (b >> 2) : b + 256 * (w >> 2)) & ~1) * 2);
    char* slab = ws + WS_SLAB + (size_t)(b >> 2) * (2 * 4 * 3 * 8192) + w * 1024;
    char* slabq = ws + WS_SLABQ + ((size_t)(b >> 2) * 8 + 2 * mw + kh) * 3072;
    u32x4 A[18][2];
    f32x4 acc[9][2];
    f32x4 xin[4];
    u32x4 gr[4];
    const int m = 16 * mtk + (l & 15);
    u32x2 xv[3], xnv[3], zo[3], kvr[3];
    unsigned zv[3][4], zf[2], hfl;
    u32x4 pp[3][3];
    const int hw = xcd * 32 + (rbl >> 1) * 8 + 4 * kh + (l & 3);
    const int ctq = (w & 1) ? 7 - qd : qd;
    auto ntile = [&](int i) { return i < 2 ? 8 * cb2 + 4 * i + sp : 32 + cb2; };
    auto granule = [&](f32x4 v) { return (u32x4){tag, pack2(v[0], v[1]), tag, pack2(v[2], v[3])}; };
    auto do_op = [&](auto K, auto Aa, auto Bb) {
      constexpr int kind = K.v, a = Aa.v, bb = Bb.v;
      if constexpr (kind == 1 || kind == 3 || kind == 4) g2_dma(K, Aa, Bb);
      if constexpr (kind == 2) gload16_sc1(A[a][bb], ws + WS_HF + ((size_t)(bb ? mtg : mtk) * 144 + kent(kh, a)) * 1024, aoff);
      if constexpr (kind == 5) gstore16_sc1(slab + ((bb * 4 + (a ^ sp)) * 3 + a - 1) * 8192, aoff, gr[a]);
      if constexpr (kind == 8) {
        if constexpr (a == 0) gload4_sc1(zf[0], ctl + FL_Z, (unsigned)(((ntile((l >> 4) & 1) * 16) & 255) + (l & 15)) * 4);
        else gload4_sc1(zf[1], ctl + FL_Z, (unsigned)(64 * cb2 + l) * 4);
      }
      if constexpr (kind == 9) {
        constexpr int i = a / 6, j = a % 6;
        const int n0 = 16 * ntile(i) + 4 * g;
        if constexpr (j == 0) gload8(xv[i], x, (unsigned)(m * D_ + n0) * 2);
        if constexpr (j == 1) gload8_sc1(xnv[i], ws + WS_XNF + ((size_t)mtk * 18 + (ntile(i) >> 1)) * 1024 + (ntile(i) & 1) * 8, aoff);
        if constexpr (j >= 2) gload4_sc1(zv[i][j - 2], ws + WS_ZT, (unsigned)((n0 + j - 2) * S_ + (m & ~1)) * 2);
      }
      if constexpr (kind == 10) gload16_sc1(pp[0][a], slab + (sp * 3 + a) * 8192, aoff);
      if constexpr (kind == 12) gload4_sc1(hfl, ctl + (a ? FL_P : FL_H), (unsigned)hw * 4);
      if constexpr (kind == 13) {
        const bool qs = a == 2 && w < 2;
        const int c = qs ? 512 + (b >> 2) : b + 256 * (w >> 2), ct = qs ? ctq : 2 * (w & 3) + (a & 1);
        gstore8_sc1(ws + WS_ZT + (size_t)c * (S_ * 2), (unsigned)(16 * (16 * ct + r) + 4 * g) * 2, zo[a]);
      }
    };
    unsigned tb[5];
#pragma unroll
    for (int t = 0; t < 4; ++t) tb[t] = lbase + kh * 4096 + trow * 128 + (((t ^ sp) ^ ((trow >> 1) & 3)) << 5) + 8 * p;
    tb[4] = lbase + kh * 1024 + trow * 32 + 8 * p;
    s16x4 blo[NQ], bhi[NQ];
    auto ld_lo = [&](auto I) {
      constexpr int n = I.v;
      if constexpr (SC.tile[n] >= 0) {
        constexpr int bi = SC.bid[n], fo = SC.foff[n], wn = fo >> 16, pw = SC.pwin[n];
        if constexpr (wn != pw) { tb[bi] += (unsigned)((wn - pw) * 65536); pin(tb[bi]); }
        blo[n % NQ] = __builtin_amdgcn_ds_read_tr16_b64_v4i16((LDSP(s16x4))(tb[bi] + (fo & 65535)));
      }
    };
    auto ld_hi = [&](auto I) {
      constexpr int n = I.v;
      if constexpr (SC.tile[n] >= 0) {
        constexpr int bi = SC.bid[n], fo = SC.foff[n];
        bhi[n % NQ] = __builtin_amdgcn_ds_read_tr16_b64_v4i16((LDSP(s16x4))(tb[bi] + (fo & 65535) + (bi < 4 ? 2048 : 512)));
      }
    };
    const char* img = sm + CONV0 + (w >> 2) * CONV_CH;
    const char* im2 = sm + CONV0 + 2 * CONV_CH;
    const int la = (r & 3) * CS + 4088 - 8 * (r >> 2) + 16 * g, lb = HB + 480 + 32 * r + 16 * g;
    const char* pa1 = img + la - 32 * (4 * (w & 3) + 1) - 64 - 3584;
    const char* pb1 = img + lb - 32 * (4 * (w & 3) + 1) - 64;
    const char* paq = im2 + la - 32 * (2 * w + 1) - 3584;
    const char* pbq = im2 + lb - 32 * (2 * w + 1);
    f32x4 ca[10];
    u32x2 fal[3][8], fah[3][8];
    bf16x8 fb[3][12];
    auto cv_load = [&](auto J, auto H) {
      constexpr int j = J.v;
      if constexpr (j < NCV) {
        constexpr Cv cv = CV.p[j];
        if constexpr (cv.newa) {
          const char* pa = (cv.part == 0 ? paq : pa1 + (cv.part == 1 ? 64 : 0)) + 512 * (7 - cv.a);
          if constexpr (H.v == 0) fal[cv.part][cv.a] = *(const u32x2*)pa; else fah[cv.part][cv.a] = *(const u32x2*)(pa + 8);
        }
        if constexpr (cv.newb && H.v == 1) {
          if constexpr (cv.part == 0) {
            const int hi = cv.b < 8 ? 7 - qd : qd;
            fb[0][cv.b] = *(const bf16x8*)(cv.a <= hi ? pbq + 512 * (hi - cv.a) : sm + CONVZ + aoff);
          } else fb[cv.part][cv.b] = *(const bf16x8*)(pb1 + (cv.part == 1 ? 64 : 0) + 512 * cv.b);
        }
      }
    };
    auto cv_mma = [&](auto J) {
      constexpr Cv cv = CV.p[J.v];
      const u32x4 af = (u32x4){fal[cv.part][cv.a][0], fal[cv.part][cv.a][1], fah[cv.part][cv.a][0], fah[cv.part][cv.a][1]};
      if constexpr (cv.first) MFMA0(ca[cv.acc], af, fb[cv.part][cv.b]); else MFMA(ca[cv.acc], af, fb[cv.part][cv.b]);
    };
    constexpr int CVL = 4;
    auto zfin = [&](f32x4 sv, u32x2 kv2, float dval) {
      const float k0 = hi2f(kv2[1]), k1 = lo2f(kv2[1]), k2 = hi2f(kv2[0]), k3 = lo2f(kv2[0]);
      return (u32x2){pack2(rb(sv[0]) + rb(k0 * dval), rb(sv[1]) + rb(k1 * dval)), pack2(rb(sv[2]) + rb(k2 * dval), rb(sv[3]) + rb(k3 * dval))};
    };
    f32x4 ps[2][4];
    char* xw = sm + G2_XOFF + w * 4096 + aoff;
    const char* xr = sm + G2_XOFF + (w ^ 4) * 4096 + aoff;
    VMWAIT(SC1.after_side + SC1.nst + 1);
    LBAR();
    STAMP(6); STAMP(7); STAMP(8);
    static_for<0, CVL>([&](auto J) { cv_load(J, IC<0>{}); SCHED(); cv_load(J, IC<1>{}); SCHED(); });
    static_for<0, NSTEP>([&](auto I) {
      constexpr int n = I.v;
      constexpr int ks = SC.ks[n], tile = SC.tile[n], sy = SC.sync[n], ev = SC.ev[n];
      constexpr int c0 = n < G2_NPRE ? 2 * n : n + G2_NPRE;
      if constexpr (n == G2_NPRE) SUB(3, 0);
      if constexpr (n == SC.cvlast) SUB(3, 1);
      if constexpr (n == SC.pbar) SUB(3, 2);
      if constexpr (n == SC.zbar) SUB(3, 3);
      if constexpr (n == SC.xbar) SUB(3, 4);
      if constexpr (n == SC.cbar) SUB(3, 5);
      if constexpr (sy >= 0) {
        static_assert(sy < 64, "vmcnt");
        VMWAIT(sy);
        static_for<SC.alo[n], SC.ahi[n]>([&](auto K) { pin(A[K.v][0]); pin(A[K.v][1]); });
      }
      if constexpr (n == SC.h1c) { VMWAIT(SC.h1wait); pin(hfl); pin(dvr); if (!__all(hfl == tag)) wait_flags(ctl, FL_H + hw, tag); }
      if constexpr (n == SC.hbar) { pin(hfl); if (!__all(hfl == tag)) wait_flags(ctl, FL_P + hw, tag); }
      if constexpr (n == SC.xbar || n == SC.pbar || n == SC.eslot[0]) asm volatile("s_waitcnt lgkmcnt(0)" ::: "memory");
      if constexpr (n == SC.cbar) {
        pin(zf[0]); pin(zf[1]);
        if (!__all(zf[0] == tag && zf[1] == tag)) {
#pragma nounroll
          for (int i = 0; i < 4; ++i) wait_flags(ctl, FL_Z + 64 * i + l, tag);
        }
      }
      if constexpr (sy >= -1) { BAR(); SCHED(); }
      if constexpr (n == SC.h2bar) { if (tid == 0) set_flag(ctl + FL_P + hslot, tag); }
      if constexpr (n == SC.zbar) { if (tid == 0) set_flag(ctl + FL_Z + b, tag); }
      const bf16x8 bfr = __builtin_bit_cast(bf16x8, __builtin_shufflevector(blo[n % NQ], bhi[n % NQ], 0, 1, 2, 3, 4, 5, 6, 7));
      if constexpr (n + PD < NSTEP) ld_lo(IC<n + PD>{});
      cv_load(IC<c0 + CVL>{}, IC<0>{});
      if constexpr (ev >= 0 && ev < 4) *(f32x4*)(xw + ev * 1024) = acc[ev][1];
      if constexpr (ev >= 4 && ev < 8) xin[ev - 4] = *(const f32x4*)(xr + (ev - 4) * 1024);
      if constexpr (ev == 8) { acc[0][0] += xin[0]; pin(acc[0][0]); }
      if constexpr (ev > 8 && ev < 12) { gr[ev - 8] = granule(acc[ev - 8][0] + xin[ev - 8]); pin(gr[ev - 8]); }
      if constexpr (n == SC.cvlast - 2) {
#pragma unroll
        for (int i = 0; i < 2; ++i) kvr[i] = *(const u32x2*)(img + 3 * CS + 2 * (2044 - (16 * (16 * (2 * (w & 3) + i) + r) + 4 * g)));
        kvr[2] = *(const u32x2*)(im2 + 3 * CS + 2 * (2044 - (16 * (16 * ctq + r) + 4 * g)));
      }
      if constexpr (ev >= 20 && ev < 28) *(f32x4*)(sm + CONVP + w * 8192 + (ev - 20) * 1024 + aoff) = ca[ev - 20];
      if constexpr (ev == 28 || ev == 29) *(f32x4*)(sm + CONVQ + w * 2048 + (ev - 28) * 1024 + aoff) = ca[ev - 20];
      if constexpr (ev == 30 || ev == 31) {
#pragma unroll
        for (int ww = 0; ww < 4; ++ww) ps[ev - 30][ww] = *(const f32x4*)(sm + CONVP + ((w & 4) + ww) * 8192 + (2 * (w & 3) + ev - 30) * 1024 + aoff);
      }
      if constexpr (ev == 32 || ev == 33) {
        const unsigned d0w = __builtin_amdgcn_readlane(dvr, 0);
        zo[ev - 32] = zfin((ps[ev - 32][0] + ps[ev - 32][1]) + (ps[ev - 32][2] + ps[ev - 32][3]), kvr[ev - 32], (b & 1) ? hi2f(d0w) : lo2f(d0w));
        pin(zo[ev - 32]);
      }
      if constexpr (ev == 34) {
#pragma unroll
        for (int ww = 0; ww < 8; ++ww) ps[ww >> 2][ww & 3] = *(const f32x4*)(sm + CONVQ + ww * 2048 + (w & 1) * 1024 + aoff);
      }
      if constexpr (ev == 35) {
        const unsigned d1w = __builtin_amdgcn_readlane(dvr, 1);
        const u32x2 zq = zfin(((ps[0][0] + ps[0][1]) + (ps[0][2] + ps[0][3])) + ((ps[1][0] + ps[1][1]) + (ps[1][2] + ps[1][3])), kvr[2],
                              ((b >> 2) & 1) ? hi2f(d1w) : lo2f(d1w));
        zo[2] = w < 2 ? zq : zo[0];
        pin(zo[2]);
      }
      SCHED();
      if constexpr (tile >= 0) { if constexpr (ks == 0) MFMA0(acc[tile][0], bfr, A[ks][0]); else MFMA(acc[tile][0], bfr, A[ks][0]); }
      else cv_mma(IC<c0>{});
      SCHED();
      if constexpr (n + PD < NSTEP) ld_hi(IC<n + PD>{});
      cv_load(IC<c0 + CVL>{}, IC<1>{});
      constexpr Op op = SC.op[n];
      do_op(IC<op.kind>{}, IC<op.a>{}, IC<op.b>{});
      SCHED();
      if constexpr (tile >= 0) { if constexpr (ks == 0) MFMA0(acc[tile][1], bfr, A[ks][1]); else MFMA(acc[tile][1], bfr, A[ks][1]); }
      else { cv_load(IC<c0 + 1 + CVL>{}, IC<0>{}); SCHED(); cv_mma(IC<c0 + 1>{}); SCHED(); cv_load(IC<c0 + 1 + CVL>{}, IC<1>{}); }
      SCHED();
      if constexpr (tile >= 0 && c0 < NCV) { cv_mma(IC<c0>{}); SCHED(); }
    });
    SUB(3, 6); SUB(3, 7);
    STAMP(9);
    SUB(2, 0);
    asm volatile("s_nop 7\n\ts_nop 7" ::: "memory");
    SCHED();
    {
      char* xw2 = sm + G2_XOFF - 8192 + w * 5120 + aoff;
      const char* xr2 = sm + G2_XOFF - 8192 + (w ^ 4) * 5120 + aoff;
      static_assert(SC.slot[128 + 2] != 15 && SC.slot[128 + 3] != 15 && SC.slot[128 + 4] != 15, "slot 15 must be idle at the end");
#pragma unroll
      for (int j = 0; j < 5; ++j) *(f32x4*)(xw2 + j * 1024) = acc[4 + j][1];
      LBAR();
#pragma unroll
      for (int j = 0; j < 5; ++j) acc[4 + j][0] += *(const f32x4*)(xr2 + j * 1024);
    }
    SUB(2, 1);
#pragma unroll
    for (int j = 1; j < 4; ++j) gr[j] = granule(acc[4 + j][0]);
    static_for<1, 4>([&](auto J) { do_op(IC<5>{}, J, IC<1>{}); });
    if (mw != sp) gstore16_sc1(slabq + ((mw ^ sp) - 1) * 1024, aoff, granule(acc[8][0]));
    STAMP(10);
    SUB(2, 2);
    const bool ownq = mw == sp;
    VMWAIT(4);
    float uu[3][4];
#pragma unroll
    for (int i = 0; i < 3; ++i) {
      pin(xv[i]); pin(xnv[i]);
#pragma unroll
      for (int j = 0; j < 4; ++j) {
        pin(zv[i][j]);
        const float xx = (j & 1) ? hi2f(xv[i][j >> 1]) : lo2f(xv[i][j >> 1]);
        const float xn = (j & 1) ? hi2f(xnv[i][j >> 1]) : lo2f(xnv[i][j >> 1]);
        const float z = (m & 1) ? hi2f(zv[i][j]) : lo2f(zv[i][j]);
        uu[i][j] = rb(xx + rb(xn * z));
        pin(uu[i][j]);
      }
    }
    {
      const int j3 = l < 3 ? l : (l < 6 ? l - 3 : 0);
      const size_t po = (l >= 3 && l < 6 && ownq) ? (size_t)(slabq - ws) + j3 * 1024 + 1008 : (size_t)(slab - ws) + ((4 + sp) * 3 + j3) * 8192 + 1008;
      wait_flags((const unsigned*)ws, (int)(po >> 2), tag);
    }
#pragma nounroll
    for (int spins = 0; spins < 40000; ++spins) {
      if (spins) {
        __builtin_amdgcn_s_sleep(1);
#pragma unroll
        for (int j = 0; j < 3; ++j) gload16_sc1(pp[0][j], slab + (sp * 3 + j) * 8192, aoff);
      }
#pragma unroll
      for (int j = 0; j < 3; ++j) gload16_sc1(pp[1][j], slab + ((4 + sp) * 3 + j) * 8192, aoff);
#pragma unroll
      for (int j = 0; j < 3; ++j) gload16_sc1(pp[2][j], slabq + j * 1024, aoff);
      VMWAIT(0);
      bool ok = true;
#pragma unroll
      for (int i = 0; i < 3; ++i)
#pragma unroll
        for (int j = 0; j < 3; ++j) { pin(pp[i][j]); if (i < 2 || ownq) ok = ok && pp[i][j][0] == tag && pp[i][j][2] == tag; }
      if (__all(ok)) break;
    }
    SUB(2, 3);
#pragma unroll
    for (int i = 0; i < 3; ++i) {
      if (i < 2 || ownq) {
        float res[4];
#pragma unroll
        for (int j = 0; j < 4; ++j) {
          float s = acc[4 * i][0][j];
#pragma unroll
          for (int k = 0; k < 3; ++k) s += (j & 1) ? hi2f(pp[i][k][1 + (j & 2)]) : lo2f(pp[i][k][1 + (j & 2)]);
          res[j] = uu[i][j] + rb(s);
        }
        *(u32x2*)(out + m * D_ + 16 * ntile(i) + 4 * g) = (u32x2){pack2(res[0], res[1]), pack2(res[2], res[3])};
      }
    }
    SUB(2, 4);
    if (b == 0 && w == 0) {
#pragma nounroll
      for (int i = 0; i < 4; ++i) wait_flags(ctl, FL_Z + 64 * i + l, tag);
      if (l == 0) set_flag(ctl, tag);
    }
  }
  STAMP(11);
}

extern "C" void kernel_launch(void* const* d_in, const int* in_sizes, int n_in, void* d_out, int out_size,
                              void* d_ws, size_t ws_size, hipStream_t stream) {
  k_main<<<256, 512, 0, stream>>>((const u16*)d_in[0], (const u16*)d_in[1], (const u16*)d_in[2], (const u16*)d_in[3],
                                  (const u16*)d_in[4], (const u16*)d_in[5], (const u16*)d_in[7], (const u16*)d_in[8],
                                  (const u16*)d_in[9], (char*)d_ws, (u16*)d_out);
}
```

```cpp
#include <hip/hip_runtime.h>
#include <hip/hip_bf16.h>
#include <stdint.h>

#define CODE_LINES 296
typedef unsigned short u16;
typedef __bf16 bf16x8 __attribute__((ext_vector_type(8)));
typedef __bf16 bf16x2 __attribute__((ext_vector_type(2)));
typedef short s16x4 __attribute__((ext_vector_type(4)));
typedef float f32x4 __attribute__((ext_vector_type(4)));
typedef unsigned u32x4 __attribute__((ext_vector_type(4)));
typedef unsigned u32x2 __attribute__((ext_vector_type(2)));
#define LDSP(T) __attribute__((address_space(3))) T*
#define GASP(T) __attribute__((address_space(1))) T*

constexpr int S_ = 2048, D_ = 576, F_ = 4608;
constexpr int RP = 2064;

constexpr size_t WS_CTL = 0;
constexpr int FL_X = 256, FL_KV = 512, FL_H = 768, FL_Z = 1024, FL_P = 1280;
constexpr int FST = 32;
constexpr size_t WS_XNF = 262144;
constexpr size_t WS_R0 = WS_XNF + (size_t)S_ * D_ * 2;
constexpr size_t WS_R1 = WS_R0 + (size_t)D_ * RP * 2;
constexpr size_t WS_ZT = WS_R1 + (size_t)D_ * RP * 2;
constexpr size_t WS_HF = WS_ZT + (size_t)D_ * S_ * 2;
constexpr size_t WS_SLAB = WS_HF + (size_t)S_ * F_ * 2;
constexpr size_t WS_SLABQ = WS_SLAB + (size_t)64 * 2 * 4 * 3 * 8192;

__device__ __forceinline__ float lo2f(unsigned w) { return __uint_as_float(w << 16); }
__device__ __forceinline__ float hi2f(unsigned w) { return __uint_as_float(w & 0xffff0000u); }
__device__ __forceinline__ float rb(float f) { return (float)(__bf16)f; }
__device__ __forceinline__ unsigned pack2(float lo, float hi) {
  bf16x2 v; v[0] = (__bf16)lo; v[1] = (__bf16)hi; return __builtin_bit_cast(unsigned, v);
}
template <int N> struct IC { static constexpr int v = N; };
template <int B, int E, class Fn> __device__ __forceinline__ void static_for(Fn&& f) {
  if constexpr (B < E) { f(IC<B>{}); static_for<B + 1, E>(f); }
}

__device__ __forceinline__ void dma16(const void* sbase, unsigned voff, unsigned lds_dst) {
  unsigned keep;
  asm volatile("s_mov_b32 %0, m0\n\ts_mov_b32 m0, %3\n\ts_nop 0\n\tglobal_load_lds_dwordx4 %1, %2\n\ts_mov_b32 m0, %0"
               : "=&s"(keep) : "v"(voff), "s"(sbase), "s"(lds_dst) : "memory");
}
__device__ __forceinline__ void dma16_sc1(const void* sbase, unsigned voff, unsigned lds_dst) {
  unsigned keep;
  asm volatile("s_mov_b32 %0, m0\n\ts_mov_b32 m0, %3\n\ts_nop 0\n\tglobal_load_lds_dwordx4 %1, %2 sc1\n\ts_mov_b32 m0, %0"
               : "=&s"(keep) : "v"(voff), "s"(sbase), "s"(lds_dst) : "memory");
}
__device__ __forceinline__ void dma1(const void* sbase, unsigned v0, unsigned lds_dst) {
  asm volatile("s_mov_b32 m0, %2\n\ts_nop 0\n\tglobal_load_lds_dwordx4 %0, %1" :: "v"(v0), "s"(sbase), "s"(lds_dst) : "memory");
}
template <int OFF> __device__ __forceinline__ void gload16o_sc1(u32x4& dst, const void* sbase, unsigned voff) {
  asm volatile("global_load_dwordx4 %0, %1, %2 offset:%3 sc1" : "=v"(dst) : "v"(voff), "s"(sbase), "n"(OFF));
}
__device__ __forceinline__ void gload16(u32x4& dst, const void* sbase, unsigned voff) {
  asm volatile("global_load_dwordx4 %0, %1, %2" : "=v"(dst) : "v"(voff), "s"(sbase));
}
__device__ __forceinline__ void gload16_sc1(u32x4& dst, const void* sbase, unsigned voff) {
  asm volatile("global_load_dwordx4 %0, %1, %2 sc1" : "=v"(dst) : "v"(voff), "s"(sbase));
}
__device__ __forceinline__ void gload8(u32x2& dst, const void* sbase, unsigned voff) {
  asm volatile("s_nop 4\n\tglobal_load_dwordx2 %0, %1, %2" : "=v"(dst) : "v"(voff), "s"(sbase));
}
__device__ __forceinline__ void gload8_sc1(u32x2& dst, const void* sbase, unsigned voff) {
  asm volatile("s_nop 4\n\tglobal_load_dwordx2 %0, %1, %2 sc1" : "=v"(dst) : "v"(voff), "s"(sbase));
}
__device__ __forceinline__ void gload4_sc1(unsigned& dst, const void* sbase, unsigned voff) {
  asm volatile("s_nop 4\n\tglobal_load_dword %0, %1, %2 sc1" : "=v"(dst) : "v"(voff), "s"(sbase));
}
__device__ __forceinline__ void gstore16_sc1(void* sbase, unsigned voff, u32x4 v) {
  asm volatile("global_store_dwordx4 %0, %1, %2 sc1\n\ts_nop 1" ::"v"(voff), "v"(v), "s"(sbase) : "memory");
}
__device__ __forceinline__ void gstore8_sc1(void* sbase, unsigned voff, u32x2 v) {
  asm volatile("s_nop 4\n\tglobal_store_dwordx2 %0, %1, %2 sc1" ::"v"(voff), "v"(v), "s"(sbase) : "memory");
}
__device__ __forceinline__ void gstore8(void* sbase, unsigned voff, u32x2 v) {
  asm volatile("global_store_dwordx2 %0, %1, %2" ::"v"(voff), "v"(v), "s"(sbase) : "memory");
}
__device__ __forceinline__ void gload4(unsigned& dst, const void* sbase, unsigned voff) {
  asm volatile("s_nop 4\n\tglobal_load_dword %0, %1, %2" : "=v"(dst) : "v"(voff), "s"(sbase));
}
__device__ __forceinline__ void touch(unsigned& d, const void* sbase, unsigned voff) {
  asm volatile("s_nop 4\n\tglobal_load_dword %0, %1, %2" : "+v"(d) : "v"(voff), "s"(sbase));
}
__device__ __forceinline__ void touch_sc1(unsigned& d, const void* sbase, unsigned voff) {
  asm volatile("s_nop 4\n\tglobal_load_dword %0, %1, %2 sc1" : "+v"(d) : "v"(voff), "s"(sbase));
}
template <class T> __device__ __forceinline__ void pin(T& v) { asm volatile("" : "+v"(v)); }
#define VMWAIT(n) asm volatile("s_waitcnt vmcnt(%0)" ::"n"(n) : "memory")
#define BAR() __builtin_amdgcn_s_barrier()
#define LBAR() do { asm volatile("s_waitcnt lgkmcnt(0)" ::: "memory"); __builtin_amdgcn_s_barrier(); } while (0)
#define SCHED() __builtin_amdgcn_sched_barrier(0)
template <class TA, class TB> __device__ __forceinline__ void MFMA0(f32x4& d, const TA& a, const TB& b) {
  asm volatile("v_mfma_f32_16x16x32_bf16 %0, %1, %2, 0" : "=&v"(d) : "v"(a), "v"(b));
}
template <class TA, class TB> __device__ __forceinline__ void MFMA(f32x4& d, const TA& a, const TB& b) {
  asm volatile("v_mfma_f32_16x16x32_bf16 %0, %1, %2, %0" : "+v"(d) : "v"(a), "v"(b));
}

__device__ __forceinline__ void set_flag(unsigned* ctl, int i, unsigned tag) {
  __hip_atomic_store((GASP(unsigned))(ctl + i * FST), tag, __ATOMIC_RELAXED, __HIP_MEMORY_SCOPE_AGENT);
}
__device__ __forceinline__ void wait_flags(const unsigned* ctl, int widx, unsigned tag) {
#pragma nounroll
  for (int spins = 0; spins < 40000; ++spins) {
    const unsigned v = __hip_atomic_load((GASP(const unsigned))(ctl + widx), __ATOMIC_RELAXED, __HIP_MEMORY_SCOPE_AGENT);
    if (__all(v == tag)) break;
    __builtin_amdgcn_s_sleep(2);
  }
  asm volatile("" ::: "memory");
}
__device__ __forceinline__ void wave_sum2(float& a, float& b) {
#define DPPADD(v, ctrl, rm) v += __int_as_float(__builtin_amdgcn_update_dpp(0, __float_as_int(v), ctrl, rm, 0xf, false))
#define DPP2(ctrl, rm) DPPADD(a, ctrl, rm); DPPADD(b, ctrl, rm)
  DPP2(0x111, 0xf); DPP2(0x112, 0xf); DPP2(0x114, 0xf); DPP2(0x118, 0xf); DPP2(0x142, 0xa); DPP2(0x143, 0xc);
#undef DPP2
#undef DPPADD
  a = __int_as_float(__builtin_amdgcn_readlane(__float_as_int(a), 63));
  b = __int_as_float(__builtin_amdgcn_readlane(__float_as_int(b), 63));
}

constexpr int SLOT = 8192, PD = 3, NQ = 4;
struct Op { int kind, a, b; };
template <int NSTEP> struct Sched {
  int ks[NSTEP], tile[NSTEP], gu[NSTEP];
  Op op[NSTEP];
  int sync[NSTEP];
  int alo[NSTEP], ahi[NSTEP];
  int ev[NSTEP], eslot[NSTEP];
  int foff[NSTEP], bid[NSTEP], pwin[NSTEP];
  int slot[5 * 32];
  int npd; Op prd[40];
  int npa; Op pra[16];
  int first_wait;
  int nrest; int rest[40];
  int nst; Op strest[12];
  int xbar, cbar, hbar, h2bar, pbar, zbar, after_side, h1c, h1wait, cvlast;
  int err;
};
constexpr int NCV = 84;
struct Cfg { int NG, K1, AW, NS, KPRE, KA, PRE0, GAP, ECAD, pairs, NPRE, AW0; };
template <int NSTEP> constexpr Sched<NSTEP> make_sched(const Cfg c) {
  Sched<NSTEP> s{};
  int n = 0;
  bool isbar[NSTEP + 1] = {};
  auto add = [&](int tl, int k) { for (int g = 0; g < c.NG; ++g) { s.ks[n] = k; s.tile[n] = tl; s.gu[n] = g; s.op[n] = {0, 0, 0}; s.sync[n] = -2; s.ev[n] = -1; ++n; } };
  for (int i = 0; i < c.NPRE; ++i) add(-1, -1);
  for (int k = 0; k < c.K1; ++k) { if (n >= PD) isbar[n - PD] = true; for (int tl = 0; tl < 9; ++tl) add(tl, k); }
  const int ngrp = c.pairs ? 5 : 3, gsz = c.pairs ? 2 : 4;
  for (int gi = 0; gi < ngrp; ++gi)
    for (int k = c.K1; k < 18; ++k) {
      const int per = (c.pairs || gi == ngrp - 1) ? 4 : 2;
      if ((k - c.K1) % per == 0) isbar[n - PD] = true;
      if (gi == ngrp - 1) add(8, k); else for (int j = 0; j < gsz; ++j) add(gi * gsz + j, k);
    }
  int fu[5 * 32] = {}, lu[5 * 32] = {};
  for (int i = 0; i < 5 * 32; ++i) { fu[i] = -1; lu[i] = -1; }
  for (int i = 0; i < NSTEP; ++i) {
    const int tl = s.tile[i], k = s.ks[i];
    if (tl < 0) continue;
    const int idb = tl < 4 ? 32 + k : (tl < 8 ? 96 + k : 128 + k / 4), ida = 64 + k;
    if (fu[idb] < 0) fu[idb] = i;
    lu[idb] = i;
    if (fu[ida] < 0) fu[ida] = i;
    lu[ida] = i;
  }
  int fin[9] = {};
  for (int i = 0; i < NSTEP; ++i) if (s.ks[i] == 17) fin[s.tile[i]] = i + 1;
  if (c.pairs) isbar[fin[3] + 1] = true;
  Op ops[128] = {}; int dl[128] = {}; int no = 0;
  auto pubbar = [&](int need) { int p = need; while (p >= 0 && !isbar[p]) --p; return p; };
  auto nextbar = [&](int after) { int p = after + 1; while (p < NSTEP && !isbar[p]) ++p; return p; };
  for (int k = 0; k < 18; ++k) {
    for (int m = 0; m < 2; ++m) { ops[no] = {2, k, m}; dl[no] = pubbar(fu[64 + k] - PD); ++no; }
    ops[no] = {1, k, 0}; dl[no] = pubbar(fu[32 + k] - PD); ++no;
    ops[no] = {3, k, 0}; dl[no] = pubbar(fu[96 + k] - PD); ++no;
    if (k % 4 == 0) { ops[no] = {4, k / 4, 0}; dl[no] = pubbar(fu[128 + k / 4] - PD); ++no; }
  }
  const int nload = no;
  const int tfree = c.pairs ? fin[3] + 1 : NSTEP + 1;
  int posn[128] = {};
  int freeat[24] = {};
  auto place = [&](int pos, Op o) { while (pos < NSTEP && s.op[pos].kind != 0) ++pos; if (pos < NSTEP) s.op[pos] = o; else s.strest[s.nst++] = o; return pos; };
  auto side = [&](int pos, Op o, int deadline) { const int p = place(pos, o); if (p >= NSTEP || p >= deadline) s.err = 6; ops[no] = o; dl[no] = deadline; posn[no] = p; ++no; return p; };
  int cvend = 0;
  s.xbar = s.cbar = s.hbar = s.h2bar = s.pbar = s.zbar = -1;
  if (!c.pairs) {
    s.cvlast = NCV - c.NPRE;
    const int e1 = nextbar(s.cvlast);
    for (int j = 0; j < 10; ++j) s.ev[e1 + j] = 20 + j;
    s.pbar = nextbar(e1 + 10);
    for (int j = 0; j < 6; ++j) s.ev[s.pbar + j] = 30 + j;
    cvend = nextbar(s.pbar + 6);
    for (int sl = 10; sl < 16; ++sl) freeat[sl] = cvend;
    s.zbar = nextbar(cvend + 4);
    side(s.pbar + 3, Op{13, 0, 0}, s.zbar); side(s.pbar + 4, Op{13, 1, 0}, s.zbar); side(s.pbar + 6, Op{13, 2, 0}, s.zbar);
    s.h2bar = nextbar(c.NPRE);
    s.hbar = nextbar(nextbar(nextbar(nextbar(s.h2bar))));
    side(s.hbar - 14, Op{12, 1, 0}, s.hbar);
    s.h1c = 5;
    side(0, Op{12, 0, 0}, NSTEP + 9);
    s.eslot[0] = e1;
  }
  int npre = 0;
  for (int i = 0; i < nload; ++i) {
    const int kk = ops[i].kind == 4 ? ops[i].a * 4 : ops[i].a;
    posn[i] = -3;
    if (ops[i].kind != 2 && kk < c.KPRE) {
      const int sl = c.PRE0 + npre++;
      if (sl >= c.NS) s.err = 4;
      posn[i] = -2; ops[i].b = sl; s.slot[ops[i].kind * 32 + ops[i].a] = sl; freeat[sl] = nextbar(lu[ops[i].kind * 32 + ops[i].a]);
      s.prd[s.npd++] = ops[i];
    } else if (ops[i].kind == 2 && kk < c.KA) {
      posn[i] = -1; s.pra[s.npa++] = ops[i]; if (dl[i] < 0) s.first_wait = s.npa;
    } else if (dl[i] < 0) s.err = 2;
  }
  int prev = -1;
  for (int pos = 0; pos < NSTEP; ++pos) {
    if (s.op[pos].kind != 0) continue;
    int flo = -1, fany = -1;
    for (int sl = 0; sl < c.NS; ++sl) if (freeat[sl] <= pos) { fany = sl; if (sl < 12 && flo < 0) flo = sl; }
    const int aw = pos < s.cvlast ? c.AW0 : (pos < s.pbar ? c.AW0 + 4 : c.AW);
    int best = -1, bs = -1;
    for (int i = 0; i < nload; ++i) {
      if (posn[i] != -3) continue;
      int sl = 0;
      if (ops[i].kind == 2) {
        if (!(ops[i].a < aw || lu[64 + ops[i].a - aw] < pos)) continue;
        if ((ops[i].a >= 8 && pos < s.hbar) || pos < s.h1c) continue;
      } else { sl = lu[ops[i].kind * 32 + ops[i].a] >= tfree ? flo : fany; if (sl < 0) continue; }
      if (best < 0 || dl[i] < dl[best]) { best = i; bs = sl; }
    }
    if (best < 0) continue;
    if (pos - prev < ((dl[best] - pos < 24 * c.NG) ? 1 : c.GAP)) continue;
    if (pos >= dl[best]) s.err = 1;
    if (ops[best].kind != 2) { ops[best].b = bs; s.slot[ops[best].kind * 32 + ops[best].a] = bs; freeat[bs] = nextbar(lu[ops[best].kind * 32 + ops[best].a]); }
    s.op[pos] = ops[best]; posn[best] = pos; prev = pos;
  }
  int lastload = 0;
  for (int i = 0; i < nload; ++i) { if (posn[i] == -3) s.err = 5; if (posn[i] > lastload) lastload = posn[i]; }
  int lastside = 0;
  if (c.pairs) {
    int order[36] = {}; int ne = 0;
    for (int gi = 0; gi < 5; ++gi) {
      if (gi == 4) { for (int j = 0; j < 4; ++j) order[ne++] = 8 * 4 + j; }
      else for (int j = 0; j < 8; ++j) order[ne++] = (2 * gi + ((j >> 1) & 1)) * 4 + (j >> 2) * 2 + (j & 1);
    }
    int done_at[36] = {};
    int cur = 0, nxt = 0;
    for (; cur < 36; ++cur) {
      int at = fin[order[cur] >> 2] + 3; if (at < nxt) at = nxt;
      if (at + 2 >= NSTEP) break;
      s.ev[at] = order[cur]; s.eslot[at] = cur % 3; done_at[cur] = at + 3; nxt = at + c.ECAD;
    }
    for (int i = cur; i < 36; ++i) { s.rest[s.nrest++] = order[i]; done_at[i] = NSTEP + 1; }
    s.hbar = nextbar(done_at[15] + 20);
    for (int i = 0; i < 36; ++i) {
      const int hu = order[i], t = hu >> 2, m = (hu >> 1) & 1, hh = hu & 1;
      if (hh != 1 || (t != 8 && (t & 1) == 0)) continue;
      if (t < 4) side(done_at[i], Op{5, t >> 1, m}, s.hbar);
      else place(done_at[i], t == 8 ? Op{6, 0, m} : Op{5, t >> 1, m});
    }
    s.cbar = tfree;
    side(18 * c.K1 + 16, Op{7, 0, 0}, tfree);
    side(18 * c.K1 + 19, Op{8, 0, 0}, tfree);
    for (int j = 0; j < 8; ++j) lastside = side(tfree + 1 + 3 * j, Op{9, j, 0}, NSTEP + 9);
    for (int j = 0; j < 6; ++j) place(tfree + 26 + 2 * j, Op{14, j, 0});
  } else {
    const int f = fin[3] + 3;
    if (f < cvend) s.err = 7;
    for (int j = 0; j < 4; ++j) s.ev[f + j] = j;
    const int bq = nextbar(f + 4);
    if (bq + 12 >= NSTEP) s.err = 3;
    for (int j = 0; j < 4; ++j) { s.ev[bq + 1 + j] = 4 + j; s.ev[bq + 5 + j] = 8 + j; }
    for (int j = 1; j < 4; ++j) place(bq + 7 + j, Op{5, j, 0});
    s.xbar = bq;
    s.cbar = pubbar(NSTEP - 24);
    if (s.cbar <= s.zbar + 24) s.err = 8;
    side(s.cbar - 15, Op{8, 0, 0}, s.cbar);
    for (int j = 0; j < 2; ++j) side(s.cbar + 1 + j, Op{9, j, 0}, NSTEP + 9);
    for (int j = 0; j < 3; ++j) lastside = side(NSTEP - 4 + j, Op{10, j, 0}, NSTEP + 9);
  }
  int cum[NSTEP + 1] = {}; cum[0] = s.npa;
  for (int i = 0; i < NSTEP; ++i) cum[i + 1] = cum[i] + (s.op[i].kind != 0);
  s.after_side = cum[NSTEP] - cum[lastside] - 1;
  s.h1wait = cum[s.h1c] - 1;
  for (int p = 0; p < NSTEP; ++p) {
    if (!isbar[p]) continue;
    int idx = -1, alo = 99, ahi = -1, pi = 0;
    for (int i = 0; i < no; ++i) {
      if (posn[i] == -2) continue;
      const int my = posn[i] < 0 ? pi++ : cum[posn[i]];
      if (dl[i] == p) { if (my > idx) idx = my; if (ops[i].kind == 2) { if (ops[i].a < alo) alo = ops[i].a; if (ops[i].a + 1 > ahi) ahi = ops[i].a + 1; } }
    }
    s.sync[p] = idx >= 0 ? cum[p] - idx - 1 : ((p <= lastload || p == s.xbar || p == s.cbar || p == s.pbar) ? -1 : -2);
    s.alo[p] = alo; s.ahi[p] = ahi;
  }
  if (s.h2bar >= 0 && s.sync[s.h2bar] < 0) s.err = 9;
  int lastw[5] = {};
  for (int i = 0; i < NSTEP; ++i) {
    const int tl = s.tile[i], k = s.ks[i];
    if (tl < 0) continue;
    s.foff[i] = tl < 8 ? s.slot[(tl < 4 ? 32 : 96) + k] * SLOT + s.gu[i] * 4096
                       : s.slot[128 + k / 4] * SLOT + ((k % 4) * 2 + s.gu[i]) * 1024;
    s.bid[i] = tl < 8 ? (tl & 3) : 4;
    s.pwin[i] = lastw[s.bid[i]]; lastw[s.bid[i]] = s.foff[i] >> 16;
  }
  return s;
}
struct Cv { int part, a, b, acc, first, newa, newb; };
struct CvList { Cv p[NCV]; };
constexpr CvList make_cv() {
  CvList L{}; int n = 0;
  for (int a = 0; a < 8; ++a) {
    L.p[n++] = {0, a, a, 9, a == 0, 1, 1};
    if (a < 4) L.p[n++] = {0, a, 8 + a, 8, a == 0, 0, 1};
  }
  for (int r = 1; r < 3; ++r)
    for (int a = 0; a < 8; ++a)
      for (int bb = 0; bb < 8 - a; ++bb) L.p[n++] = {r, a, bb, a + bb, r == 1 && a == 0, bb == 0, a == 0};
  return L;
}
constexpr CvList CV = make_cv();
#define RK0 3
#define ROT1 0
#define ROT2 cb2
#define G1_K1 10
#define G2_K1 8
#define G2_GAP 2
#define G1_KPRE 3
#define G2_KA 4
constexpr Cfg C1 = {2, G1_K1, 18 - G1_K1, 20, G1_KPRE, 2, 5, 3, 4, 1, 0, 0};
#define G2_NPRE 28
#define G2_AW0 6
constexpr int NST2 = 162 + G2_NPRE;
constexpr Cfg C2 = {1, G2_K1, 18 - G2_K1 + 2, 13, 0, 0, 0, G2_GAP, 0, 0, G2_NPRE, G2_AW0};
constexpr Sched<324> SC1 = make_sched<324>(C1);
constexpr Sched<NST2> SC2 = make_sched<NST2>(C2);
static_assert(SC1.err == 0 && SC2.err == 0, "schedule");
constexpr int G2_XOFF = 16 * SLOT;
constexpr int P0_LN = 0, P0_KV = 10240, P0_K = 20480;
constexpr int CS = 4160, HB = 4 * CS, CONV_CH = HB + 4608;
constexpr int CONV0 = 12 * SLOT, CONVZ = CONV0 + 3 * CONV_CH;
constexpr int CONVP = 12 * SLOT, CONVQ = 10 * SLOT;
constexpr int ZT_LDS = 13 * SLOT;
static_assert(CONVZ + 1024 <= 163840, "conv lds");

__device__ __forceinline__ float silu_mul(float g, float u) {
  return g * __builtin_amdgcn_rcpf(1.0f + __builtin_amdgcn_exp2f(g * -1.4426950408889634f)) * u;
}
__device__ __forceinline__ bf16x8 ld2x8(const char* p) {
  const u32x2 a = *(const u32x2*)p, b = *(const u32x2*)(p + 8);
  return __builtin_bit_cast(bf16x8, (u32x4){a[0], a[1], b[0], b[1]});
}

__global__ __launch_bounds__(512, 2) void k_main(const u16* __restrict__ x, const u16* __restrict__ kk,
                                                 const u16* __restrict__ vv, const u16* __restrict__ sk,
                                                 const u16* __restrict__ hk, const u16* __restrict__ Dp,
                                                 const u16* __restrict__ w1, const u16* __restrict__ w3,
                                                 const u16* __restrict__ w2, char* __restrict__ ws,
                                                 u16* __restrict__ out) {
  __shared__ __attribute__((aligned(16))) char sm[163840];
  const unsigned lbase = (unsigned)(uintptr_t)(LDSP(char))sm;
  const int tid = threadIdx.x, l = tid & 63, b = blockIdx.x;
  const int w = __builtin_amdgcn_readfirstlane(tid >> 6);
  const int xcd = b & 7, idx = b >> 3;
  unsigned* ctl = (unsigned*)(ws + WS_CTL);
  const unsigned aoff = l * 16;
  const int g = l >> 4, q = (l >> 2) & 3, p = l & 3, trow = 4 * g + q;
  const int drow = 8 * (w & 3) + (l >> 3);
  const unsigned dsw = ((l & 7) ^ (((drow >> 1) & 3) << 1)) << 4;

  unsigned tagv;
  gload4_sc1(tagv, ctl, 0u);
  unsigned cpf = 0;
  {
    unsigned long long pc;
    asm volatile("s_getpc_b64 %0" : "=s"(pc));
    const int ln = idx * 16 + (l & 15);
    if (w == 5) touch(cpf, (const void*)pc, (unsigned)(ln < CODE_LINES ? ln : 0) * 128);
  }

  const int rbk = 4 * (xcd >> 2) + (idx & 3), cb = 8 * (xcd & 3) + (idx >> 2);
  const char* csrc1 = (const char*)((w >> 2) ? w3 : w1) + cb * 256;
  const char* lsrc1 = (const char*)((w & 1) ? w3 : w1) + (4096 + 16 * cb) * 2;
  const int rot1 = ROT1, rlim = 18 - rot1;
  const unsigned cv1 = drow * (F_ * 2) + dsw, lv1 = (unsigned)(l >> 1) * (F_ * 2) + (l & 1) * 16;
  const unsigned wl1 = lbase + w * 1024;
  const char* csA = csrc1 + (size_t)rot1 * 32 * (F_ * 2);
  const char* csB = csA - (size_t)(18 - RK0) * 32 * (F_ * 2);
  auto kr1 = [&](int k) { const int e = k + rot1; return k < RK0 ? k : (e < 18 ? e : e - (18 - RK0)); };
  auto g1_dma = [&](auto K, auto Aa, auto Sl) {
    constexpr int kind = K.v, a = Aa.v, sl = Sl.v;
    if constexpr (kind == 1 || kind == 3) dma1(a < RK0 ? csrc1 : (a < rlim ? csA : csB), cv1 + a * 32 * (F_ * 2) + (kind == 3 ? 128 : 0), wl1 + sl * SLOT);
    if constexpr (kind == 4) {
      const int wq = a < 4 ? w : (w & 3);
      dma1(lsrc1 + (size_t)kr1(4 * a + (wq >> 1)) * 32 * (F_ * 2), lv1, lbase + sl * SLOT + wq * 1024);
    }
  };
  const int sp = xcd & 3, cb2 = idx & 3, rbl = idx >> 2, rb2 = 8 * (xcd >> 2) + rbl;
  const int mw = w & 3, kh = w >> 2;
  auto kent = [&](int half, int k) {
    return k < 16 ? 32 * sp + 16 * half + 4 * ((((k & 7) >> 1) + ROT2) & 3) + (k & 1) + 2 * (k >> 3) : 112 + 4 * sp + 2 * half + k;
  };
  auto prod = [&](int j) { return 4 * ((j + ROT2) & 3); };
  const char* cbase2 = (const char*)w2 + cb2 * 256 + (size_t)(1024 * sp + 512 * kh) * (D_ * 2);
  const unsigned cv2 = drow * (D_ * 2) + dsw;
  auto g2_dma = [&](auto K, auto Aa, auto Sl) {
    constexpr int kind = K.v, a = Aa.v, sl = Sl.v;
    if constexpr (kind == 1 || kind == 3) {
      const int ks = a < 16 ? prod((a & 7) >> 1) + (a & 1) + 2 * (a >> 3) : 112 - 28 * sp - 14 * kh + a;
      dma1(cbase2, cv2 + (unsigned)(ks * 32 * (D_ * 2) + (kind == 3 ? 128 : 0)), wl1 + sl * SLOT);
    }
    if constexpr (kind == 4) {
      const int wq = a < 4 ? w : (w & 3);
      dma1((const char*)w2 + 1024 + cb2 * 32 + (size_t)kent(wq & 1, 4 * a + (wq >> 1)) * 32 * (D_ * 2),
           (unsigned)(l >> 1) * (D_ * 2) + (l & 1) * 16, lbase + sl * SLOT + wq * 1024);
    }
  };

  if (w >= 4) __builtin_amdgcn_s_setprio(1);
  unsigned tag;
  {
    u32x4 xa, skv;
    unsigned xe;
    const char* xrow = (const char*)x + (size_t)(8 * b + w) * (D_ * 2);
    gload16(xa, xrow, aoff);
    gload4(xe, xrow + 1024, (l & 31) * 4);
    const int tbk = b >> 3, cbk = b & 7, t0 = 64 * tbk;
    const int kt = tid < 396 ? tid : 0, cp = kt % 36, tg = kt / 36;
    BAR();
#pragma unroll
    for (int j = 0; j < 3; ++j) {
      int pi = j * 7 + (w < 7 ? w : 0); if (pi >= 20) pi -= 1;
      const int isv = pi >= 10, ci = (pi - 10 * isv) * 64 + l, row = ci / 9;
      int tt = t0 - (isv ? 2 : 5) + row; tt = tt < 0 ? 0 : (tt > S_ - 1 ? S_ - 1 : tt);
      dma16(isv ? vv : kk, (unsigned)(tt * D_ + 72 * cbk + (ci - row * 9) * 8) * 2, lbase + P0_K + pi * 1024);
    }
    gload16(skv, sk, (unsigned)(72 * cbk + 2 * cp) * 8);
    constexpr int NPD = SC1.npd, NB1 = 5;
    VMWAIT(4);
    pin(xa); pin(xe); pin(tagv); pin(cpf);
    {
      float v[10];
#pragma unroll
      for (int j = 0; j < 4; ++j) { v[2 * j] = lo2f(xa[j]); v[2 * j + 1] = hi2f(xa[j]); }
      v[8] = lo2f(xe); v[9] = hi2f(xe);
      const float pv = __int_as_float(__builtin_amdgcn_readfirstlane(__float_as_int(v[0])));
      float d[10];
#pragma unroll
      for (int j = 0; j < 10; ++j) d[j] = v[j] - pv;
      if (l >= 32) { d[8] = 0.f; d[9] = 0.f; }
      float s1 = ((d[0] + d[1]) + (d[2] + d[3])) + ((d[4] + d[5]) + (d[6] + d[7])) + (d[8] + d[9]);
      float qa = d[0] * d[0], qb = d[1] * d[1];
#pragma unroll
      for (int j = 2; j < 10; j += 2) { qa = __builtin_fmaf(d[j], d[j], qa); qb = __builtin_fmaf(d[j + 1], d[j + 1], qb); }
      float s2 = qa + qb;
      wave_sum2(s1, s2);
      const float m1 = s1 * (1.0f / D_), var = __builtin_fmaxf(__builtin_fmaf(-m1, m1, s2 * (1.0f / D_)), 0.f);
      const float mb = rb(pv + m1), sd = rb(__builtin_amdgcn_sqrtf(rb(rb(var) + 1e-5f))), rs = __builtin_amdgcn_rcpf(sd);
      unsigned o[5];
#pragma unroll
      for (int j = 0; j < 5; ++j) {
        const unsigned dd = pack2(v[2 * j] - mb, v[2 * j + 1] - mb);
        const float d0 = lo2f(dd), d1 = hi2f(dd);
        float q0 = d0 * rs, q1 = d1 * rs;
        q0 = __builtin_fmaf(__builtin_fmaf(-q0, sd, d0), rs, q0);
        q1 = __builtin_fmaf(__builtin_fmaf(-q1, sd, d1), rs, q1);
        o[j] = pack2(q0, q1);
      }
      const int cq = l & 3;
      char* d0 = sm + P0_LN + (l >> 2) * 512 + (2 * (cq & 1)) * 128 + w * 16 + (cq >> 1) * 8;
      *(u32x2*)d0 = (u32x2){o[0], o[1]};
      *(u32x2*)(d0 + 128) = (u32x2){o[2], o[3]};
      if (l < 32) *(unsigned*)(sm + P0_LN + 8192 + (l >> 4) * 512 + ((l >> 1) & 3) * 128 + w * 16 + ((l >> 3) & 1) * 8 + (l & 1) * 4) = o[4];
    }
    tag = __builtin_amdgcn_readfirstlane(tagv) + 1u;
    LBAR();
    if (w == 7) {
      char* xdst = ws + WS_XNF + (size_t)(b >> 1) * 18432;
      u32x4 pc[9];
#pragma unroll
      for (int i = 0; i < 9; ++i) pc[i] = *(const u32x4*)(sm + P0_LN + i * 1024 + aoff);
#pragma unroll
      for (int i = 0; i < 9; ++i) gstore16_sc1(xdst + i * 2048, (unsigned)(l >> 3) * 256 + (l & 7) * 16 + (b & 1) * 128, pc[i]);
      BAR();
      VMWAIT(0);
      if (l == 0) set_flag(ctl, FL_X + b, tag);
    }
    static_for<0, NB1>([&](auto I) { constexpr Op o = SC1.prd[I.v]; g1_dma(IC<o.kind>{}, IC<o.a>{}, IC<o.b>{}); });
    VMWAIT(NB1);
    pin(skv);
    if (w != 7) BAR();
    if (tid < 396) {
      const unsigned* kp = (const unsigned*)(sm + P0_K) + (6 * tg) * 36 + cp;
      const unsigned* vp = (const unsigned*)(sm + P0_K + 10240) + (6 * tg) * 36 + cp;
      unsigned kd[9], vd[6];
#pragma unroll
      for (int i = 0; i < 9; ++i) kd[i] = kp[i * 36];
#pragma unroll
      for (int i = 0; i < 6; ++i) vd[i] = vp[i * 36];
      if (tbk == 0 && tg == 0) {
#pragma unroll
        for (int i = 0; i < 5; ++i) kd[i] = 0u;
        vd[0] = 0u; vd[1] = 0u;
      }
      unsigned* T = (unsigned*)(sm + P0_KV) + (2 * cp) * 33 + 32 - 3 * tg;
#pragma unroll
      for (int e = 0; e < 2; ++e) {
        const float c0 = lo2f(skv[2 * e]), c1 = hi2f(skv[2 * e]), c2 = lo2f(skv[2 * e + 1]), c3 = hi2f(skv[2 * e + 1]);
        float kf[9], r[6];
#pragma unroll
        for (int i = 0; i < 9; ++i) kf[i] = e ? hi2f(kd[i]) : lo2f(kd[i]);
#pragma unroll
        for (int u = 0; u < 6; ++u) {
          const float a = __builtin_fmaf(kf[u + 3], c0, __builtin_fmaf(kf[u + 2], c1, __builtin_fmaf(kf[u + 1], c2, kf[u] * c3)));
          r[u] = rb(a) * (e ? hi2f(vd[u]) : lo2f(vd[u]));
        }
#pragma unroll
        for (int u = 0; u < 3; ++u) T[e * 33 - u] = pack2(r[2 * u + 1], r[2 * u]);
      }
    }
    LBAR();
    {
      const unsigned* T = (const unsigned*)(sm + P0_KV);
      const int u0 = 1984 - t0;
      char* r0 = ws + WS_R0 + (size_t)(72 * cbk) * (RP * 2) + u0 * 2;
      char* r1 = ws + WS_R1 + (size_t)(72 * cbk) * (RP * 2) + u0 * 2;
#pragma nounroll
      for (int id = tid; id < 576; id += 512) {
        const int r = id >> 3, ch = id & 7;
        unsigned d[5];
#pragma unroll
        for (int j = 0; j < 5; ++j) d[j] = T[r * 33 + ch * 4 + j];
        gstore16_sc1(r0, (unsigned)r * (RP * 2) + ch * 16, (u32x4){d[0], d[1], d[2], d[3]});
        gstore16_sc1(r1, (unsigned)r * (RP * 2) + ch * 16,
                     (u32x4){__builtin_amdgcn_alignbyte(d[1], d[0], 2), __builtin_amdgcn_alignbyte(d[2], d[1], 2),
                             __builtin_amdgcn_alignbyte(d[3], d[2], 2), __builtin_amdgcn_alignbyte(d[4], d[3], 2)});
      }
      if (tbk == 0 && tid < 72) {
        gstore16_sc1(r0, (unsigned)tid * (RP * 2) + 128, (u32x4){0u, 0u, 0u, 0u});
        gstore16_sc1(r1, (unsigned)tid * (RP * 2) + 128, (u32x4){0u, 0u, 0u, 0u});
      }
    }
    static_for<NB1, NPD>([&](auto I) { constexpr Op o = SC1.prd[I.v]; g1_dma(IC<o.kind>{}, IC<o.a>{}, IC<o.b>{}); });
  }

  u32x2 xv[3], xnv[3];
  const int mtk = 8 * rb2 + 2 * mw + kh, mtg = 8 * rb2 + 2 * mw + 1 - kh;
  const int nt0 = __builtin_amdgcn_readfirstlane(8 * cb2 + sp);
  auto ntile = [&](int i) { return i < 2 ? nt0 + 4 * i : 32 + cb2; };
  const int hslot = xcd * 32 + (idx & 3) * 8 + (idx >> 2);
  const int kvw = w < 4 ? FL_KV + ((b + 256 * (l >> 5)) / 72) * 32 + (l & 31) : (l < 32 ? FL_KV + 7 * 32 + l : FL_X + 16 * rb2 + (l & 15));
  {
#define SC SC1
    constexpr int NSTEP = 324;
    const int mt0 = 16 * rbk + 2 * w;
    wait_flags(ctl, (FL_X + 2 * mt0 + (l & 3)) * FST, tag);
    const char* asrc = ws + WS_XNF + (size_t)mt0 * 18432;
    const char* asA = asrc + rot1 * 1024;
    const char* asB = asA - (18 - RK0) * 1024;
    char* hdst = ws + WS_HF + ((size_t)mt0 * 144 + 4 * cb) * 1024;
    char* ldst = ws + WS_HF + ((size_t)mt0 * 144 + 128 + (cb >> 1)) * 1024 + (cb & 1) * 8;
    u32x4 A[18][2];
    unsigned pk[9][2][2];
    unsigned fl0 = 0, fl1;
    auto do_op = [&](auto K, auto Aa, auto Bb) {
      constexpr int kind = K.v, a = Aa.v, bb = Bb.v;
      if constexpr (kind == 1 || kind == 3 || kind == 4) g1_dma(K, Aa, Bb);
      if constexpr (kind == 2) gload16o_sc1<(a & 3) * 1024>(A[a][bb], a < RK0 ? asrc : (a < rlim ? asA : asB), aoff + (a >> 2) * 4096 + bb * 18432);
      if constexpr (kind == 5) gstore16_sc1(hdst + (bb * 144 + a) * 1024, aoff, (u32x4){pk[2 * a][bb][0], pk[2 * a][bb][1], pk[2 * a + 1][bb][0], pk[2 * a + 1][bb][1]});
      if constexpr (kind == 6) gstore8_sc1(ldst + bb * 144 * 1024, aoff, (u32x2){pk[8][bb][0], pk[8][bb][1]});
      if constexpr (kind == 7) {
        const int ln = idx * 324 + (tid < 324 ? tid : 0), row = ln / 9;
        touch(fl0, w2, (unsigned)((row < 1024 ? 1024 * sp + row : 3072 + 128 * sp + row) * 9 + (ln - row * 9)) * 128);
      }
      if constexpr (kind == 14) {
        constexpr int i = a >> 1;
        if constexpr ((a & 1) == 0) gload8(xv[i], x, (unsigned)((16 * mtk + (l & 15)) * D_ + 16 * ntile(i) + 4 * g) * 2);
        else gload8_sc1(xnv[i], ws + WS_XNF + ((size_t)mtk * 18 + (ntile(i) >> 1)) * 1024 + (ntile(i) & 1) * 8, aoff);
      }
      if constexpr (kind == 8) gload4_sc1(fl1, ctl, (unsigned)kvw * (4 * FST));
      if constexpr (kind == 9) {
        if constexpr (a < 6) {
          const int c = a < 4 ? b + 256 * (a >> 1) : 512 + (b >> 2), sg = 3 - (w >> 1), part = (w & 1) * 2 + (a & 1);
          dma16_sc1(ws + ((sg & 1) ? WS_R1 : WS_R0) + (size_t)c * (RP * 2) + (sg >> 1) * 4 + part * 1024, aoff,
                    lbase + CONV0 + (a >> 1) * CONV_CH + (w >> 1) * CS + part * 1024);
        } else {
          const int ci = a == 6 ? (w >> 2) : 2, c = a == 6 ? b + 256 * (w >> 2) : 512 + (b >> 2);
          dma16((const char*)hk + (size_t)c * (S_ * 2) + (w & 3) * 1024, aoff, lbase + CONV0 + ci * CONV_CH + HB + 480 + (w & 3) * 1024);
        }
      }
    };
    static_for<0, SC.npa>([&](auto I) { constexpr Op o = SC.pra[I.v]; do_op(IC<o.kind>{}, IC<o.a>{}, IC<o.b>{}); });
    unsigned tb[5];
#pragma unroll
    for (int t = 0; t < 4; ++t) tb[t] = lbase + trow * 128 + ((t ^ ((trow >> 1) & 3)) << 5) + 8 * p;
    tb[4] = lbase + trow * 32 + 8 * p;
    f32x4 acc[9][2][2];
    s16x4 blo[NQ], bhi[NQ];
    auto ld_lo = [&](auto I) {
      constexpr int n = I.v; constexpr int bi = SC.bid[n], fo = SC.foff[n], wn = fo >> 16, pw = SC.pwin[n];
      if constexpr (wn != pw) { tb[bi] += (unsigned)((wn - pw) * 65536); pin(tb[bi]); }
      blo[n % NQ] = __builtin_amdgcn_ds_read_tr16_b64_v4i16((LDSP(s16x4))(tb[bi] + (fo & 65535)));
    };
    auto ld_hi = [&](auto I) {
      constexpr int n = I.v; constexpr int bi = SC.bid[n], fo = SC.foff[n];
      bhi[n % NQ] = __builtin_amdgcn_ds_read_tr16_b64_v4i16((LDSP(s16x4))(tb[bi] + (fo & 65535) + (bi < 4 ? 2048 : 512)));
    };
    float et[3][2];
    auto epi_stage = [&](auto I, auto H) {
      constexpr int n = I.v, slot2 = H.v;
      static_for<0, 3>([&](auto Dd) {
        constexpr int d = Dd.v;
        if constexpr (n - d >= 0) {
          constexpr int ep = SC.ev[n - d];
          if constexpr (ep >= 0) {
            constexpr int t = ep >> 2, m = (ep >> 1) & 1, hh = ep & 1, stg = 2 * d + slot2;
            const float g0 = acc[t][m][0][2 * hh], g1 = acc[t][m][0][2 * hh + 1];
            float& e0 = et[SC.eslot[n - d]][0]; float& e1 = et[SC.eslot[n - d]][1];
            if constexpr (stg == 0) { e0 = g0 * -1.4426950408889634f; pin(e0); e1 = g1 * -1.4426950408889634f; }
            if constexpr (stg == 1) { e0 = __builtin_amdgcn_exp2f(e0); e1 = __builtin_amdgcn_exp2f(e1); }
            if constexpr (stg == 2) { e0 = 1.0f + e0; pin(e0); e1 = 1.0f + e1; }
            if constexpr (stg == 3) { e0 = __builtin_amdgcn_rcpf(e0); e1 = __builtin_amdgcn_rcpf(e1); }
            if constexpr (stg == 4) { e0 *= g0; pin(e0); e1 *= g1; pin(e1); }
            if constexpr (stg == 5) { e0 *= acc[t][m][1][2 * hh]; pin(e0); e1 *= acc[t][m][1][2 * hh + 1]; pk[t][m][hh] = pack2(e0, e1); }
          }
        }
      });
    };
    VMWAIT(SC.npa - SC.first_wait);
    pin(A[0][0]); pin(A[0][1]);
    LBAR();
    if (tid == 0) set_flag(ctl, FL_KV + (b & 7) * 32 + (b >> 3), tag);
    static_for<0, PD>([&](auto I) { ld_lo(I); ld_hi(I); });
    SCHED();
    static_for<0, NSTEP>([&](auto I) {
      constexpr int n = I.v;
      constexpr int ks = SC.ks[n], tile = SC.tile[n], gu = SC.gu[n], sy = SC.sync[n];
      if constexpr (sy >= 0) {
        static_assert(sy < 64, "vmcnt");
        VMWAIT(sy);
        static_for<SC.alo[n], SC.ahi[n]>([&](auto K) { pin(A[K.v][0]); pin(A[K.v][1]); });
      }
      if constexpr (n == SC.cbar) { pin(fl0); pin(fl1); if (!__all(fl1 == tag)) wait_flags(ctl, (kvw) * FST, tag); }
      if constexpr (sy >= -1) { BAR(); SCHED(); }
      if constexpr (n == SC.hbar) { if (tid == 0) set_flag(ctl, FL_H + hslot, tag); }
      const bf16x8 bfr = __builtin_bit_cast(bf16x8, __builtin_shufflevector(blo[n % NQ], bhi[n % NQ], 0, 1, 2, 3, 4, 5, 6, 7));
      if constexpr (n + PD < NSTEP) ld_lo(IC<n + PD>{});
      epi_stage(I, IC<0>{});
      SCHED();
      if constexpr (ks == 0) MFMA0(acc[tile][0][gu], bfr, A[ks][0]); else MFMA(acc[tile][0][gu], bfr, A[ks][0]);
      SCHED();
      if constexpr (n + PD < NSTEP) ld_hi(IC<n + PD>{});
      constexpr Op op = SC.op[n];
      do_op(IC<op.kind>{}, IC<op.a>{}, IC<op.b>{});
      epi_stage(I, IC<1>{});
      SCHED();
      if constexpr (ks == 0) MFMA0(acc[tile][1][gu], bfr, A[ks][1]); else MFMA(acc[tile][1][gu], bfr, A[ks][1]);
      SCHED();
    });
    asm volatile("s_nop 7\n\ts_nop 7" ::: "memory");
    SCHED();
    static_for<0, SC.nrest>([&](auto I) {
      constexpr int ep = SC.rest[I.v], t = ep >> 2, m = (ep >> 1) & 1, hh = ep & 1;
      pk[t][m][hh] = pack2(silu_mul(acc[t][m][0][2 * hh], acc[t][m][1][2 * hh]), silu_mul(acc[t][m][0][2 * hh + 1], acc[t][m][1][2 * hh + 1]));
    });
    static_for<0, SC.nst>([&](auto I) { constexpr Op o = SC.strest[I.v]; do_op(IC<o.kind>{}, IC<o.a>{}, IC<o.b>{}); });
  }

#undef SC
#define SC SC2
  {
    constexpr int NSTEP = NST2;
    const int qd = b & 3, r = l & 15;
    if (tid < 178) {
      const int o = tid < 24 ? CONV0 + (tid >> 3) * CONV_CH + ((tid >> 1) & 3) * CS + 4096 + (tid & 1) * 16
                             : (tid < 114 ? CONV0 + ((tid - 24) / 30) * CONV_CH + HB + ((tid - 24) % 30) * 16 : CONVZ + (tid - 114) * 16);
      *(u32x4*)(sm + o) = (u32x4){0u, 0u, 0u, 0u};
    }
    unsigned dvr;
    gload4(dvr, Dp, (unsigned)(((l & 1) ? 512 + (b >> 2) : b + 256 * (w >> 2)) & ~1) * 2);
    char* slab = ws + WS_SLAB + (size_t)(b >> 2) * (2 * 4 * 3 * 8192) + w * 1024;
    char* slabq = ws + WS_SLABQ + ((size_t)(b >> 2) * 8 + 2 * mw + kh) * 3072;
    const char* hbk = ws + WS_HF + ((size_t)mtk * 144 + 32 * sp + 16 * kh) * 1024;
    const char* hbg = ws + WS_HF + ((size_t)mtg * 144 + 32 * sp + 16 * kh) * 1024;
    u32x4 A[18][2];
    f32x4 acc[9][2];
    f32x4 xin[4];
    u32x4 gr[4];
    const int m = 16 * mtk + (l & 15);
    u32x2 zo[3], kvr[3];
    unsigned zf0, hfl;
    const int zw = FL_Z + ((l & 31) < 4 ? ((128 * cb2 + 16 * sp + 16 * (w & 4)) & 255) + 4 * (w & 3) + (l & 3) : 64 * cb2 + 16 * (w & 3) + (l & 15));
    u32x4 pp[3][3];
    const int hw = xcd * 32 + (rbl >> 1) * 8 + 4 * kh + (l & 3);
    const int ctq = (w & 1) ? 7 - qd : qd;
    auto granule = [&](f32x4 v) { return (u32x4){tag, pack2(v[0], v[1]), tag, pack2(v[2], v[3])}; };
    auto do_op = [&](auto K, auto Aa, auto Bb) {
      constexpr int kind = K.v, a = Aa.v, bb = Bb.v;
      if constexpr (kind == 1 || kind == 3 || kind == 4) g2_dma(K, Aa, Bb);
      if constexpr (kind == 2) {
        if constexpr (a < 16) gload16o_sc1<((a & 1) + 2 * (a >> 3)) * 1024>(A[a][bb], (bb ? hbg : hbk) + prod((a & 7) >> 1) * 1024, aoff);
        else gload16o_sc1<(a - 16) * 1024>(A[a][bb], (bb ? hbg : hbk) + (128 - 28 * sp - 14 * kh) * 1024, aoff);
      }
      if constexpr (kind == 5) gstore16_sc1(slab + ((bb * 4 + (a ^ sp)) * 3 + a - 1) * 8192, aoff, gr[a]);
      if constexpr (kind == 8) gload4_sc1(zf0, ctl, (unsigned)zw * (4 * FST));
      if constexpr (kind == 9) {
        const int pc = a == 0 ? w : 8 + (w & 3);
        dma16_sc1(ws + WS_ZT + (size_t)(16 * ntile(pc >> 2) + 4 * (pc & 3)) * (S_ * 2) + rb2 * 256, (unsigned)(l >> 4) * (S_ * 2) + (l & 15) * 16,
                  lbase + ZT_LDS + pc * 1024);
      }
      if constexpr (kind == 10) gload16_sc1(pp[0][a], slab + (sp * 3 + a) * 8192, aoff);
      if constexpr (kind == 12) gload4_sc1(hfl, ctl, (unsigned)((a ? FL_P : FL_H) + hw) * (4 * FST));
      if constexpr (kind == 13) {
        const bool qs = a == 2 && w < 2;
        const int c = qs ? 512 + (b >> 2) : b + 256 * (w >> 2), ct = qs ? ctq : 2 * (w & 3) + (a & 1);
        gstore8_sc1(ws + WS_ZT + (size_t)c * (S_ * 2), (unsigned)(16 * (16 * ct + r) + 4 * g) * 2, zo[a]);
      }
    };
    unsigned tb[5];
#pragma unroll
    for (int t = 0; t < 4; ++t) tb[t] = lbase + kh * 4096 + trow * 128 + (((t ^ sp) ^ ((trow >> 1) & 3)) << 5) + 8 * p;
    tb[4] = lbase + kh * 1024 + trow * 32 + 8 * p;
    s16x4 blo[NQ], bhi[NQ];
    auto ld_lo = [&](auto I) {
      constexpr int n = I.v;
      if constexpr (SC.tile[n] >= 0) {
        constexpr int bi = SC.bid[n], fo = SC.foff[n], wn = fo >> 16, pw = SC.pwin[n];
        if constexpr (wn != pw) { tb[bi] += (unsigned)((wn - pw) * 65536); pin(tb[bi]); }
        blo[n % NQ] = __builtin_amdgcn_ds_read_tr16_b64_v4i16((LDSP(s16x4))(tb[bi] + (fo & 65535)));
      }
    };
    auto ld_hi = [&](auto I) {
      constexpr int n = I.v;
      if constexpr (SC.tile[n] >= 0) {
        constexpr int bi = SC.bid[n], fo = SC.foff[n];
        bhi[n % NQ] = __builtin_amdgcn_ds_read_tr16_b64_v4i16((LDSP(s16x4))(tb[bi] + (fo & 65535) + (bi < 4 ? 2048 : 512)));
      }
    };
    const char* img = sm + CONV0 + (w >> 2) * CONV_CH;
    const char* im2 = sm + CONV0 + 2 * CONV_CH;
    const int la = (r & 3) * CS + 4088 - 8 * (r >> 2) + 16 * g, lb = HB + 480 + 32 * r + 16 * g;
    const char* pa1 = img + la - 32 * (4 * (w & 3) + 1) - 64 - 3584;
    const char* pb1 = img + lb - 32 * (4 * (w & 3) + 1) - 64;
    const char* paq = im2 + la - 32 * (2 * w + 1) - 3584;
    const char* pbq = im2 + lb - 32 * (2 * w + 1);
    f32x4 ca[10];
    u32x2 fal[3][8], fah[3][8];
    bf16x8 fb[3][12];
    auto cv_load = [&](auto J, auto H) {
      constexpr int j = J.v;
      if constexpr (j < NCV) {
        constexpr Cv cv = CV.p[j];
        if constexpr (cv.newa) {
          const char* pa = (cv.part == 0 ? paq : pa1 + (cv.part == 1 ? 64 : 0)) + 512 * (7 - cv.a);
          if constexpr (H.v == 0) fal[cv.part][cv.a] = *(const u32x2*)pa; else fah[cv.part][cv.a] = *(const u32x2*)(pa + 8);
        }
        if constexpr (cv.newb && H.v == 1) {
          if constexpr (cv.part == 0) {
            const int hi = cv.b < 8 ? 7 - qd : qd;
            fb[0][cv.b] = *(const bf16x8*)(cv.a <= hi ? pbq + 512 * (hi - cv.a) : sm + CONVZ + aoff);
          } else fb[cv.part][cv.b] = *(const bf16x8*)(pb1 + (cv.part == 1 ? 64 : 0) + 512 * cv.b);
        }
      }
    };
    auto cv_mma = [&](auto J) {
      constexpr Cv cv = CV.p[J.v];
      const u32x4 af = (u32x4){fal[cv.part][cv.a][0], fal[cv.part][cv.a][1], fah[cv.part][cv.a][0], fah[cv.part][cv.a][1]};
      if constexpr (cv.first) MFMA0(ca[cv.acc], af, fb[cv.part][cv.b]); else MFMA(ca[cv.acc], af, fb[cv.part][cv.b]);
    };
    constexpr int CVL = 4;
    auto zfin = [&](f32x4 sv, u32x2 kv2, float dval) {
      const float k0 = hi2f(kv2[1]), k1 = lo2f(kv2[1]), k2 = hi2f(kv2[0]), k3 = lo2f(kv2[0]);
      return (u32x2){pack2(rb(sv[0]) + rb(k0 * dval), rb(sv[1]) + rb(k1 * dval)), pack2(rb(sv[2]) + rb(k2 * dval), rb(sv[3]) + rb(k3 * dval))};
    };
    f32x4 ps[2][4];
    char* xw = sm + G2_XOFF + w * 4096 + aoff;
    const char* xr = sm + G2_XOFF + (w ^ 4) * 4096 + aoff;
    VMWAIT(SC1.after_side + SC1.nst + 1);
    LBAR();
    static_for<0, CVL>([&](auto J) { cv_load(J, IC<0>{}); SCHED(); cv_load(J, IC<1>{}); SCHED(); });
    static_for<0, NSTEP>([&](auto I) {
      constexpr int n = I.v;
      constexpr int ks = SC.ks[n], tile = SC.tile[n], sy = SC.sync[n], ev = SC.ev[n];
      constexpr int c0 = n < G2_NPRE ? 2 * n : n + G2_NPRE;
      if constexpr (sy >= 0) {
        static_assert(sy < 64, "vmcnt");
        VMWAIT(sy);
        static_for<SC.alo[n], SC.ahi[n]>([&](auto K) { pin(A[K.v][0]); pin(A[K.v][1]); });
      }
      if constexpr (n == SC.h1c) { VMWAIT(SC.h1wait); pin(hfl); pin(dvr); if (!__all(hfl == tag)) wait_flags(ctl, (FL_H + hw) * FST, tag); }
      if constexpr (n == SC.hbar) { pin(hfl); if (!__all(hfl == tag)) wait_flags(ctl, (FL_P + hw) * FST, tag); }
      if constexpr (n == SC.xbar || n == SC.pbar || n == SC.eslot[0]) asm volatile("s_waitcnt lgkmcnt(0)" ::: "memory");
      if constexpr (n == SC.cbar) {
        pin(zf0);
        if (!__all(zf0 == tag)) wait_flags(ctl, zw * FST, tag);
      }
      if constexpr (sy >= -1) { BAR(); SCHED(); }
      if constexpr (n == SC.h2bar) { if (tid == 0) set_flag(ctl, FL_P + hslot, tag); }
      if constexpr (n == SC.zbar) { if (tid == 0) set_flag(ctl, FL_Z + b, tag); }
      const bf16x8 bfr = __builtin_bit_cast(bf16x8, __builtin_shufflevector(blo[n % NQ], bhi[n % NQ], 0, 1, 2, 3, 4, 5, 6, 7));
      if constexpr (n + PD < NSTEP) ld_lo(IC<n + PD>{});
      cv_load(IC<c0 + CVL>{}, IC<0>{});
      if constexpr (ev >= 0 && ev < 4) *(f32x4*)(xw + ev * 1024) = acc[ev][1];
      if constexpr (ev >= 4 && ev < 8) xin[ev - 4] = *(const f32x4*)(xr + (ev - 4) * 1024);
      if constexpr (ev == 8) { acc[0][0] += xin[0]; pin(acc[0][0]); }
      if constexpr (ev > 8 && ev < 12) { gr[ev - 8] = granule(acc[ev - 8][0] + xin[ev - 8]); pin(gr[ev - 8]); }
      if constexpr (n == SC.cvlast - 2) {
#pragma unroll
        for (int i = 0; i < 2; ++i) kvr[i] = *(const u32x2*)(img + 3 * CS + 2 * (2044 - (16 * (16 * (2 * (w & 3) + i) + r) + 4 * g)));
        kvr[2] = *(const u32x2*)(im2 + 3 * CS + 2 * (2044 - (16 * (16 * ctq + r) + 4 * g)));
      }
      if constexpr (ev >= 20 && ev < 28) *(f32x4*)(sm + CONVP + w * 8192 + (ev - 20) * 1024 + aoff) = ca[ev - 20];
      if constexpr (ev == 28 || ev == 29) *(f32x4*)(sm + CONVQ + w * 2048 + (ev - 28) * 1024 + aoff) = ca[ev - 20];
      if constexpr (ev == 30 || ev == 31) {
#pragma unroll
        for (int ww = 0; ww < 4; ++ww) ps[ev - 30][ww] = *(const f32x4*)(sm + CONVP + ((w & 4) + ww) * 8192 + (2 * (w & 3) + ev - 30) * 1024 + aoff);
      }
      if constexpr (ev == 32 || ev == 33) {
        const unsigned d0w = __builtin_amdgcn_readlane(dvr, 0);
        zo[ev - 32] = zfin((ps[ev - 32][0] + ps[ev - 32][1]) + (ps[ev - 32][2] + ps[ev - 32][3]), kvr[ev - 32], (b & 1) ? hi2f(d0w) : lo2f(d0w));
        pin(zo[ev - 32]);
      }
      if constexpr (ev == 34) {
#pragma unroll
        for (int ww = 0; ww < 8; ++ww) ps[ww >> 2][ww & 3] = *(const f32x4*)(sm + CONVQ + ww * 2048 + (w & 1) * 1024 + aoff);
      }
      if constexpr (ev == 35) {
        const unsigned d1w = __builtin_amdgcn_readlane(dvr, 1);
        const u32x2 zq = zfin(((ps[0][0] + ps[0][1]) + (ps[0][2] + ps[0][3])) + ((ps[1][0] + ps[1][1]) + (ps[1][2] + ps[1][3])), kvr[2],
                              ((b >> 2) & 1) ? hi2f(d1w) : lo2f(d1w));
        zo[2] = w < 2 ? zq : zo[0];
        pin(zo[2]);
      }
      SCHED();
      if constexpr (tile >= 0) { if constexpr (ks == 0) MFMA0(acc[tile][0], bfr, A[ks][0]); else MFMA(acc[tile][0], bfr, A[ks][0]); }
      else cv_mma(IC<c0>{});
      SCHED();
      if constexpr (n + PD < NSTEP) ld_hi(IC<n + PD>{});
      cv_load(IC<c0 + CVL>{}, IC<1>{});
      constexpr Op op = SC.op[n];
      do_op(IC<op.kind>{}, IC<op.a>{}, IC<op.b>{});
      SCHED();
      if constexpr (tile >= 0) { if constexpr (ks == 0) MFMA0(acc[tile][1], bfr, A[ks][1]); else MFMA(acc[tile][1], bfr, A[ks][1]); }
      else { cv_load(IC<c0 + 1 + CVL>{}, IC<0>{}); SCHED(); cv_mma(IC<c0 + 1>{}); SCHED(); cv_load(IC<c0 + 1 + CVL>{}, IC<1>{}); }
      SCHED();
      if constexpr (tile >= 0 && c0 < NCV) { cv_mma(IC<c0>{}); SCHED(); }
    });

    asm volatile("s_nop 7\n\ts_nop 7" ::: "memory");
    SCHED();
    {
      char* xw2 = sm + G2_XOFF - 8192 + w * 5120 + aoff;
      const char* xr2 = sm + G2_XOFF - 8192 + (w ^ 4) * 5120 + aoff;
      static_assert(SC.slot[128 + 2] != 15 && SC.slot[128 + 3] != 15 && SC.slot[128 + 4] != 15, "slot 15 must be idle at the end");
#pragma unroll
      for (int j = 0; j < 5; ++j) *(f32x4*)(xw2 + j * 1024) = acc[4 + j][1];
      VMWAIT(3);
      LBAR();
#pragma unroll
      for (int j = 0; j < 5; ++j) acc[4 + j][0] += *(const f32x4*)(xr2 + j * 1024);
    }
#pragma unroll
    for (int j = 1; j < 4; ++j) gr[j] = granule(acc[4 + j][0]);
    static_for<1, 4>([&](auto J) { do_op(IC<5>{}, J, IC<1>{}); });
    if (mw != sp) gstore16_sc1(slabq + ((mw ^ sp) - 1) * 1024, aoff, granule(acc[8][0]));

    const bool ownq = mw == sp;
    float uu[3][4];
#pragma unroll
    for (int i = 0; i < 3; ++i) {
      pin(xv[i]); pin(xnv[i]);
#pragma unroll
      for (int j = 0; j < 4; ++j) {
        const float xx = (j & 1) ? hi2f(xv[i][j >> 1]) : lo2f(xv[i][j >> 1]);
        const float xn = (j & 1) ? hi2f(xnv[i][j >> 1]) : lo2f(xnv[i][j >> 1]);
        const float z = lo2f(*(const u16*)(sm + ZT_LDS + i * 4096 + (4 * g + j) * 256 + ((2 * mw + kh) * 16 + r) * 2));
        uu[i][j] = rb(xx + rb(xn * z));
        pin(uu[i][j]);
      }
    }
    {
      const int j3 = l < 3 ? l : (l < 6 ? l - 3 : 0);
      const size_t po = (l >= 3 && l < 6 && ownq) ? (size_t)(slabq - ws) + j3 * 1024 + 1008 : (size_t)(slab - ws) + ((4 + sp) * 3 + j3) * 8192 + 1008;
      wait_flags((const unsigned*)ws, (int)(po >> 2), tag);
    }
#pragma nounroll
    for (int spins = 0; spins < 40000; ++spins) {
      if (spins) {
        __builtin_amdgcn_s_sleep(1);
#pragma unroll
        for (int j = 0; j < 3; ++j) gload16_sc1(pp[0][j], slab + (sp * 3 + j) * 8192, aoff);
      }
#pragma unroll
      for (int j = 0; j < 3; ++j) gload16_sc1(pp[1][j], slab + ((4 + sp) * 3 + j) * 8192, aoff);
#pragma unroll
      for (int j = 0; j < 3; ++j) gload16_sc1(pp[2][j], slabq + j * 1024, aoff);
      VMWAIT(0);
      bool ok = true;
#pragma unroll
      for (int i = 0; i < 3; ++i)
#pragma unroll
        for (int j = 0; j < 3; ++j) { pin(pp[i][j]); if (i < 2 || ownq) ok = ok && pp[i][j][0] == tag && pp[i][j][2] == tag; }
      if (__all(ok)) break;
    }
#pragma unroll
    for (int i = 0; i < 3; ++i) {
      if (i < 2 || ownq) {
        float res[4];
#pragma unroll
        for (int j = 0; j < 4; ++j) {
          float s = acc[4 * i][0][j];
#pragma unroll
          for (int k = 0; k < 3; ++k) s += (j & 1) ? hi2f(pp[i][k][1 + (j & 2)]) : lo2f(pp[i][k][1 + (j & 2)]);
          res[j] = uu[i][j] + rb(s);
        }
        gstore8_sc1(out, (unsigned)(m * D_ + 16 * ntile(i) + 4 * g) * 2, (u32x2){pack2(res[0], res[1]), pack2(res[2], res[3])});
      }
    }
    if (b == 0 && w == 0) {
#pragma nounroll
      for (int i = 0; i < 4; ++i) wait_flags(ctl, (FL_Z + 64 * i + l) * FST, tag);
      if (l == 0) set_flag(ctl, 0, tag);
    }
  }
}

extern "C" void kernel_launch(void* const* d_in, const int* in_sizes, int n_in, void* d_out, int out_size,
                              void* d_ws, size_t ws_size, hipStream_t stream) {
  k_main<<<256, 512, 0, stream>>>((const u16*)d_in[0], (const u16*)d_in[1], (const u16*)d_in[2], (const u16*)d_in[3],
                                  (const u16*)d_in[4], (const u16*)d_in[5], (const u16*)d_in[7], (const u16*)d_in[8],
                                  (const u16*)d_in[9], (char*)d_ws, (u16*)d_out);
}
```
